```python
import torch

def _softmax(x, axis=-1):
    return torch.softmax(x, dim=axis)
def _gelu(x):
    return torch.nn.functional.gelu(x, approximate="tanh")
def _silu(x):
    return torch.nn.functional.silu(x)
def _sigmoid(x):
    return torch.sigmoid(x)
def _layernorm(x, axis=-1, eps=1e-5):
    m = x.mean(dim=axis, keepdim=True)
    v = x.var(dim=axis, keepdim=True, unbiased=False)
    return (x - m) / torch.sqrt(v + eps)
def _scatter_add(tgt, idx, src, axis=0):
    return tgt.clone().index_add_(axis, idx, src)
def _segment_sum(src, seg, n):
    out = torch.zeros((n,) + src.shape[1:], dtype=src.dtype)
    return out.index_add_(0, seg, src)
def _pad(x, pw):
    flat = tuple(v for pair in reversed(pw) for v in pair)
    return torch.nn.functional.pad(x, flat)

kernel_name = 'sequential_preLN__ssm_conv_s4d__mixer_channel_mlp__prompt'

def setup_inputs():

    _w = (lambda a: (lambda t: t.to(torch.bfloat16) if t.is_floating_point() else t)(torch.as_tensor(a)))
    def _A_setup():
        Bsz, L, D, N = 1, 2048, 512, 128
        return {
            "u": _w((lambda *s: torch.randn(*s, dtype=torch.bfloat16) * 0.05)(Bsz, L, D)),
            "log_A_real": _w((lambda *s: torch.randn(*s, dtype=torch.bfloat16) * 3.2)(D, N) * 0.5),
            "A_imag": _w((lambda *s: torch.randn(*s, dtype=torch.bfloat16) * 0.05)(D, N) * 3.14),
            "C_real": _w((lambda *s: torch.randn(*s, dtype=torch.bfloat16) * 0.1)(D, N) / N ** 0.5),
            "C_imag": _w((lambda *s: torch.randn(*s, dtype=torch.bfloat16) * 0.2)(D, N) / N ** 0.5),
            "log_dt": _w((lambda *s: torch.randn(*s, dtype=torch.bfloat16) * 0.05)(D) * 0.5 - 4.0),
            "D_skip": _w((lambda *s: torch.randn(*s, dtype=torch.bfloat16) * 0.05)(D)),
        }
    _A_in = _A_setup()

    def _B_setup():
        B, S, D, F = 1, 2048, 512, 1376
        return {
            "x": _w((lambda *s: torch.randn(*s, dtype=torch.bfloat16) * 0.05)(B, S, D)),
            "w1": _w((lambda *s: torch.randn(*s, dtype=torch.bfloat16) * 1.6)(D, F) * (D ** -0.5)),
            "b1": _w((lambda *s: torch.randn(*s, dtype=torch.bfloat16) * 0.8)(F)),
            "w2": _w((lambda *s: torch.randn(*s, dtype=torch.bfloat16) * 0.05)(F, D) * (F ** -0.5)),
            "b2": _w((lambda *s: torch.randn(*s, dtype=torch.bfloat16) * 0.05)(D)),
        }
    _B_in = _B_setup()

    return {
        "A_u": _A_in["u"],
        "A_log_A_real": _A_in["log_A_real"],
        "A_A_imag": _A_in["A_imag"],
        "A_C_real": _A_in["C_real"],
        "A_C_imag": _A_in["C_imag"],
        "A_log_dt": _A_in["log_dt"],
        "A_D_skip": _A_in["D_skip"],
        "B_x": _B_in["x"],
        "B_w1": _B_in["w1"],
        "B_b1": _B_in["b1"],
        "B_w2": _B_in["w2"],
        "B_b2": _B_in["b2"],
    }

def reference(A_u, A_log_A_real, A_A_imag, A_C_real, A_C_imag, A_log_dt, A_D_skip, B_x, B_w1, B_b1, B_w2, B_b2):
    def _A_ref(u, log_A_real, A_imag, C_real, C_imag, log_dt, D_skip):
        L = u.shape[1]
        dt = torch.exp(log_dt)
        A = -torch.exp(log_A_real) + 1j * A_imag
        C = (C_real + 1j * C_imag) * dt[:, None]
        pos = (lambda x, dt: x.to(dt))(torch.arange(L), log_dt.dtype)
        P = torch.exp((A * dt[:, None])[:, :, None] * pos[None, None, :])
        K = 2.0 * torch.einsum("dn,dnl->dl", C, P).real
        L2 = 2 * L
        u_t = (lambda x, *d: x.permute(*d))(u, 0, 2, 1)
        Kf = (lambda x, *a, **k: torch.fft.rfft(x.float(), *a, **k))(K, L2)
        Uf = (lambda x, *a, **k: torch.fft.rfft(x.float(), *a, **k))(u_t, L2)
        y = (lambda x, *a, **k: torch.fft.irfft(x, *a, **k).to(torch.bfloat16))(Uf * Kf[None, :, :], L2)[:, :, :L]
        y = (lambda x, *d: x.permute(*d))(y, 0, 2, 1)
        return y + u * D_skip[None, None, :]

    def _B_ref(x, w1, b1, w2, b2):
        y = _layernorm(x, axis=-1)
        h = _gelu(torch.einsum("bsd,df->bsf", y, w1) + b1)
        return torch.einsum("bsf,fd->bsd", h, w2) + b2

    x = A_u
    h = x + _A_ref(_layernorm(x, axis=-1), A_log_A_real, A_A_imag, A_C_real, A_C_imag, A_log_dt, A_D_skip)
    h2 = h + _B_ref(_layernorm(h, axis=-1), B_w1, B_b1, B_w2, B_b2)
    return h2
```

```cpp
#include <hip/hip_runtime.h>
#include <hip/hip_bf16.h>
#include <stdint.h>

typedef unsigned short u16;
typedef __attribute__((ext_vector_type(8))) _Float16 f16x8;
typedef __attribute__((ext_vector_type(2))) _Float16 f16x2;
typedef __attribute__((ext_vector_type(8))) __bf16 bf16x8;
typedef __attribute__((ext_vector_type(2))) __bf16 bf16x2;
typedef __attribute__((ext_vector_type(2))) float f32x2;
typedef __attribute__((ext_vector_type(4))) float f32x4;
typedef __attribute__((ext_vector_type(4))) unsigned u32x4;
typedef __attribute__((ext_vector_type(2))) unsigned u32x2;
typedef __attribute__((ext_vector_type(4))) short s16x4;
#define LDS_AS __attribute__((address_space(3)))

#define SEQ 2048
#define DM 512
#define NM 128
#define FF 1376
#define NFT 86
#define NKS2 43

#define UT_PITCH 4160
#define WS_UT 0
#define WS_XT (WS_UT + DM * UT_PITCH)
#define KR_PITCH 4608
#define WS_KR (WS_XT + DM * 4096)
#define WS_KSC (WS_KR + DM * KR_PITCH)
#define WS_W1T (WS_KSC + DM * 4)
#define WS_W2T (WS_W1T + NFT * 16 * 1024)
#define WS_CPART (WS_W2T + 32 * NKS2 * 1024)
#define WS_CSUM (WS_CPART + 16 * FF * 4)
#define WS_HT (WS_CSUM + 5632)
#define WS_HACT (WS_HT + DM * 4096)
#define WS_END (WS_HACT + 128 * NKS2 * 1024)

__device__ __forceinline__ float bf2f(unsigned v) { return __uint_as_float(v << 16); }
__device__ __forceinline__ float bflo(unsigned v) { return __uint_as_float(v << 16); }
__device__ __forceinline__ float bfhi(unsigned v) { return __uint_as_float(v & 0xffff0000u); }
__device__ __forceinline__ unsigned pk_bf16(float a, float b) {
  f32x2 v = {a, b};
  return __builtin_bit_cast(unsigned, __builtin_convertvector(v, bf16x2));
}
__device__ __forceinline__ unsigned pk_f16(float a, float b) {
  f32x2 v = {a, b};
  return __builtin_bit_cast(unsigned, __builtin_convertvector(v, f16x2));
}
__device__ __forceinline__ float rbf(float a) { return bflo(pk_bf16(a, a)); }
__device__ __forceinline__ float h2f_lo(unsigned v) { return (float)__builtin_bit_cast(f16x2, v)[0]; }
__device__ __forceinline__ float h2f_hi(unsigned v) { return (float)__builtin_bit_cast(f16x2, v)[1]; }

struct cpx { float r, i; };
__device__ __forceinline__ cpx cmul(cpx a, cpx b) {
  cpx o; o.r = a.r * b.r - a.i * b.i; o.i = a.r * b.i + a.i * b.r; return o;
}
__device__ __forceinline__ cpx cexp_dir(float lr2, float fr, float e) {
  float mag = __builtin_amdgcn_exp2f(lr2 * e);
  float ph = __builtin_amdgcn_fractf(fr * e);
  cpx o; o.r = mag * __builtin_amdgcn_cosf(ph); o.i = mag * __builtin_amdgcn_sinf(ph); return o;
}

#define K1_AROW 528
#define K1_IMG (32 * K1_AROW)
#define K1_LDS_A 0
#define K1_LDS_B (2 * K1_IMG)
#define K1_LDS_KC (4 * K1_IMG)
#define K1_LDS_RED (K1_LDS_KC + 2 * 1296)
#define K1_LDS_SIZE (K1_LDS_RED + 512)

__global__ __launch_bounds__(256) void k1_prep(const u16* __restrict__ x, const u16* __restrict__ lar,
                                               const u16* __restrict__ aim, const u16* __restrict__ cre,
                                               const u16* __restrict__ cim, const u16* __restrict__ ldt,
                                               const u16* __restrict__ w1, const u16* __restrict__ w2,
                                               char* __restrict__ ws) {
  __shared__ __attribute__((aligned(16))) char smem[K1_LDS_SIZE];
  const int b = blockIdx.x, tid = threadIdx.x, w = tid >> 6, l = tid & 63;
  const int g = l >> 4, l15 = l & 15;

  const int rb = (b & 7) * 32 + (b >> 3);
  unsigned xv[8];
  float piv[8];
  {
    const unsigned* xr = (const unsigned*)(x + rb * 8 * DM + 128 * w + 2 * l);
#pragma unroll
    for (int r = 0; r < 8; ++r) xv[r] = xr[r * 256];
#pragma unroll
    for (int r = 0; r < 8; ++r) piv[r] = bf2f(x[(rb * 8 + r) * DM]);
  }
  const int c = tid >> 7, n = tid & 127, d = 2 * b + c;
  const float p_lar = bf2f(lar[d * NM + n]), p_aim = bf2f(aim[d * NM + n]);
  const float p_cr = bf2f(cre[d * NM + n]), p_ci = bf2f(cim[d * NM + n]);
  const float p_ldt = bf2f(ldt[d]);

  const int gw = b * 4 + w;
  u16 wv[3][8];
#pragma unroll
  for (int it = 0; it < 3; ++it) {
    int fi = gw + it * 1024;
    if (fi < 1376) {
      int ks = fi / NFT, ft = fi - ks * NFT;
      const u16* src = w1 + (size_t)(32 * ks + 8 * g) * FF + 16 * ft + l15;
#pragma unroll
      for (int j = 0; j < 8; ++j) wv[it][j] = src[j * FF];
    } else if (fi < 2752) {
      int f2 = fi - 1376;
      int ks = f2 >> 5, nt = f2 & 31;
      const u16* src = w2 + (size_t)(32 * ks + 4 * g) * DM + 16 * nt + l15;
#pragma unroll
      for (int j = 0; j < 8; ++j) wv[it][j] = src[((j & 3) + 16 * (j >> 2)) * DM];
    }
  }

  float* red = (float*)(smem + K1_LDS_RED);

  {
    float s1[8], s2[8];
#pragma unroll
    for (int r = 0; r < 8; ++r) {
      float a = bflo(xv[r]) - piv[r], bb = bfhi(xv[r]) - piv[r];
      s1[r] = a + bb;
      s2[r] = a * a + bb * bb;
    }
#pragma unroll
    for (int o = 32; o > 0; o >>= 1) {
#pragma unroll
      for (int r = 0; r < 8; ++r) {
        s1[r] += __shfl_xor(s1[r], o);
        s2[r] += __shfl_xor(s2[r], o);
      }
    }
    if (l == 0) {
#pragma unroll
      for (int r = 0; r < 8; ++r) { red[(w * 8 + r) * 2] = s1[r]; red[(w * 8 + r) * 2 + 1] = s2[r]; }
    }
    float cm = fmaxf(fabsf(p_cr), fabsf(p_ci));
#pragma unroll
    for (int o = 32; o > 0; o >>= 1) cm = fmaxf(cm, __shfl_xor(cm, o));
    if (l == 0) red[64 + w] = cm;
  }
  __syncthreads();

  {
    unsigned ub[8];
#pragma unroll
    for (int r = 0; r < 8; ++r) {
      float t1 = red[r * 2] + red[(8 + r) * 2] + red[(16 + r) * 2] + red[(24 + r) * 2];
      float t2 = red[r * 2 + 1] + red[(8 + r) * 2 + 1] + red[(16 + r) * 2 + 1] + red[(24 + r) * 2 + 1];
      float dm = t1 * (1.f / 512.f);
      float var = fmaxf(t2 * (1.f / 512.f) - dm * dm, 0.f);
      float mb = rbf(piv[r] + dm);
      float den = rbf(sqrtf(rbf(rbf(var) + 1e-5f)));
      float rd = 1.f / den;
      unsigned dd = pk_bf16(bflo(xv[r]) - mb, bfhi(xv[r]) - mb);
      unsigned uu = pk_bf16(bflo(dd) * rd, bfhi(dd) * rd);
      ub[r] = pk_f16(bflo(uu), bfhi(uu));
    }
    u32x4 u0, u1, x0, x1;
#pragma unroll
    for (int q = 0; q < 4; ++q) {
      u0[q] = __builtin_amdgcn_perm(ub[2 * q + 1], ub[2 * q], 0x05040100u);
      u1[q] = __builtin_amdgcn_perm(ub[2 * q + 1], ub[2 * q], 0x07060302u);
      x0[q] = __builtin_amdgcn_perm(xv[2 * q + 1], xv[2 * q], 0x05040100u);
      x1[q] = __builtin_amdgcn_perm(xv[2 * q + 1], xv[2 * q], 0x07060302u);
    }
    const int ch = 128 * w + 2 * l;
    char* up = ws + WS_UT + (size_t)ch * UT_PITCH + 32 + rb * 16;
    char* xp = ws + WS_XT + (size_t)ch * 4096 + rb * 16;
    *(u32x4*)up = u0;
    *(u32x4*)(up + UT_PITCH) = u1;
    *(u32x4*)xp = x0;
    *(u32x4*)(xp + 4096) = x1;
    if (tid < 8) {
      u32x4 z = {0, 0, 0, 0};
      int cc = 2 * b + (tid >> 2), q = tid & 3;
      *(u32x4*)(ws + WS_UT + (size_t)cc * UT_PITCH + (q < 2 ? q * 16 : 4128 + (q - 2) * 16)) = z;
    }
  }

#pragma unroll
  for (int it = 0; it < 3; ++it) {
    int fi = gw + it * 1024;
    if (fi < 2752) {
      u32x4 o;
#pragma unroll
      for (int q = 0; q < 4; ++q) o[q] = (unsigned)wv[it][2 * q] | ((unsigned)wv[it][2 * q + 1] << 16);
      if (fi < 1376) {
        int ks = fi / NFT, ft = fi - ks * NFT;
        *(u32x4*)(ws + WS_W1T + (size_t)(ft * 16 + ks) * 1024 + l * 16) = o;
        float s = 0.f;
#pragma unroll
        for (int q = 0; q < 4; ++q) s += bflo(o[q]) + bfhi(o[q]);
        s += __shfl_xor(s, 16);
        s += __shfl_xor(s, 32);
        if (l < 16) ((float*)(ws + WS_CPART))[ks * FF + 16 * ft + l] = s;
      } else {
        int f2 = fi - 1376;
        int ks = f2 >> 5, nt = f2 & 31;
        *(u32x4*)(ws + WS_W2T + (size_t)(nt * NKS2 + ks) * 1024 + l * 16) = o;
      }
    }
  }

  const float dt = rbf(__expf(p_ldt));
  const float cmax = fmaxf(red[64 + 2 * c], red[64 + 2 * c + 1]);
  {
    const float ea = rbf(__expf(p_lar));
    const float lr2 = (-ea * dt) * 1.4426950408889634f;
    const float fr = (p_aim * dt) * 0.15915494309189535f;
    const float cinv = cmax > 0.f ? 1.f / cmax : 0.f;
    char* Aimg = smem + K1_LDS_A + c * K1_IMG + n * 4;
    char* Bimg = smem + K1_LDS_B + c * K1_IMG + n * 4;
    cpx z8 = cexp_dir(lr2, fr, 8.f);
    cpx P; P.r = p_cr * cinv; P.i = p_ci * cinv;
#pragma unroll
    for (int a = 0; a < 32; ++a) {
      *(unsigned*)(Aimg + a * K1_AROW) = pk_f16(P.r, P.i);
      P = cmul(P, z8);
    }
    cpx z[8];
    z[0].r = 1.f; z[0].i = 0.f;
    z[1] = cexp_dir(lr2, fr, 1.f);
#pragma unroll
    for (int k = 2; k < 8; ++k) z[k] = cmul(z[k - 1], z[1]);
    cpx e256 = cexp_dir(lr2, fr, 256.f), e1024 = cexp_dir(lr2, fr, 1024.f);
    cpx e512 = cmul(e256, e256), e768 = cmul(e512, e256);
#define PUTB(col, v) { cpx _t = (v); *(unsigned*)(Bimg + (col) * K1_AROW) = pk_f16(_t.r, -_t.i); }
#pragma unroll
    for (int k = 0; k < 8; ++k) PUTB(k, z[k]);
    PUTB(8, e256); PUTB(9, cmul(e256, z[2])); PUTB(10, cmul(e256, z[4])); PUTB(11, cmul(e256, z[6]));
    PUTB(12, e512); PUTB(13, cmul(e512, z[4])); PUTB(14, e768); PUTB(15, cmul(e768, z[4]));
    PUTB(16, e1024); PUTB(17, cmul(e1024, e256)); PUTB(18, cmul(e1024, e512)); PUTB(19, cmul(e1024, e768));
    PUTB(20, cmul(e1024, e1024));
#pragma unroll
    for (int k = 21; k < 32; ++k) *(unsigned*)(Bimg + k * K1_AROW) = 0u;
#undef PUTB
  }
  __syncthreads();

  {
    const int cc = w >> 1, rt = w & 1;
    const char* Ab = smem + K1_LDS_A + cc * K1_IMG + (16 * rt + l15) * K1_AROW + g * 16;
    const char* Bb = smem + K1_LDS_B + cc * K1_IMG + l15 * K1_AROW + g * 16;
    f32x4 acc0 = {0, 0, 0, 0}, acc1 = {0, 0, 0, 0};
#pragma unroll
    for (int q = 0; q < 8; ++q) {
      f16x8 a = *(const f16x8*)(Ab + q * 64);
      f16x8 b0 = *(const f16x8*)(Bb + q * 64);
      f16x8 b1 = *(const f16x8*)(Bb + 16 * K1_AROW + q * 64);
      acc0 = __builtin_amdgcn_mfma_f32_16x16x32_f16(a, b0, acc0, 0, 0, 0);
      acc1 = __builtin_amdgcn_mfma_f32_16x16x32_f16(a, b1, acc1, 0, 0, 0);
    }
    _Float16* kc = (_Float16*)(smem + K1_LDS_KC + cc * 1296);
#pragma unroll
    for (int r = 0; r < 4; ++r) {
      int a = 16 * rt + 4 * g + r;
      int col = l15;
      int idx;
      if (col < 8) idx = 8 * a + col;
      else if (col < 12) idx = 256 + 4 * a + (col - 8);
      else idx = 384 + 64 * ((col - 12) >> 1) + 2 * a + (col & 1);
      kc[idx] = (_Float16)(acc0[r] * (1.f / 128.f));
      if (col < 4) kc[512 + 32 * col + a] = (_Float16)(acc1[r] * (1.f / 128.f));
      else if (col == 4 && a == 0) kc[640] = (_Float16)(acc1[r] * (1.f / 128.f));
    }
  }
  __syncthreads();

  for (int it = tid; it < 2 * 275; it += 256) {
    int cc = it >= 275, ck = it - cc * 275;
    const u16* kc = (const u16*)(smem + K1_LDS_KC + cc * 1296);
    unsigned v[8];
#pragma unroll
    for (int j = 0; j < 8; ++j) {
      int lg = 2072 - 8 * ck - j;
      unsigned u = __float_as_uint((float)lg);
      u += 0x7fffu + ((u >> 16) & 1u);
      int idx = lg < 256 ? lg : (int)(u >> 16) - 0x4380 + 256;
      bool ok = lg >= 0 && lg < SEQ;
      v[j] = ok ? (unsigned)kc[ok ? idx : 0] : 0u;
    }
    u32x4 o;
#pragma unroll
    for (int q = 0; q < 4; ++q) o[q] = v[2 * q] | (v[2 * q + 1] << 16);
    *(u32x4*)(ws + WS_KR + (size_t)(2 * b + cc) * KR_PITCH + ck * 16) = o;
  }
  if (n == 0) ((float*)(ws + WS_KSC))[d] = 2.f * dt * cmax * 128.f;
}

#define K2_ROWB 288
#define K2_COPY (18 * K2_ROWB)
#define K2_UCH (4 * K2_COPY)
#define K2_LDS_U 0
#define K2_LDS_K (2 * K2_UCH)
#define K2_LDS_Y (K2_LDS_K + 2 * 4608)
#define K2_YW (32 * 68 * 4)
#define K2_LDS_SIZE (K2_LDS_Y + 4 * K2_YW)

__global__ __launch_bounds__(256) void k2_conv(const u16* __restrict__ dsk, char* __restrict__ ws) {
  __shared__ __attribute__((aligned(16))) char smem[K2_LDS_SIZE];
  const int b = blockIdx.x, tid = threadIdx.x, w = tid >> 6, l = tid & 63;
  const int g = l >> 4, l15 = l & 15;

  u32x4 ld[10];
#pragma unroll
  for (int it = 0; it < 10; ++it) {
    int i = tid + it * 256;
    int cs = i / 306, rem = i - cs * 306;
    int row = rem / 17, q = rem - row * 17;
    int cc = cs >> 2, s = cs & 3;
    int e0 = 128 * (row - 2) + 8 * q + s;
    u32x4 v = {0, 0, 0, 0};
    if (i < 2448 && e0 >= -16) {
      const char* src = ws + WS_UT + (size_t)(2 * b + cc) * UT_PITCH + 32 + e0 * 2;
      __builtin_memcpy(&v, src, 16);
    }
    ld[it] = v;
  }
  u32x4 lk[3];
#pragma unroll
  for (int it = 0; it < 3; ++it) {
    int i = tid + it * 256;
    int cc = i >= 275, ck = i - cc * 275;
    u32x4 v = {0, 0, 0, 0};
    if (i < 550) v = *(const u32x4*)(ws + WS_KR + (size_t)(2 * b + cc) * KR_PITCH + ck * 16);
    lk[it] = v;
  }
  u32x4 lx[2];
#pragma unroll
  for (int it = 0; it < 2; ++it) lx[it] = *(const u32x4*)(ws + WS_XT + (size_t)(2 * b + it) * 4096 + tid * 16);
  const float ksc0 = ((const float*)(ws + WS_KSC))[2 * b], ksc1 = ((const float*)(ws + WS_KSC))[2 * b + 1];
  const float dk0 = bf2f(dsk[2 * b]), dk1 = bf2f(dsk[2 * b + 1]);
  if (b < 6) {
    int f = b * 256 + tid;
    if (f < FF) {
      const float* cp = (const float*)(ws + WS_CPART);
      float s = 0.f;
#pragma unroll
      for (int k = 0; k < 16; ++k) s += cp[k * FF + f];
      ((float*)(ws + WS_CSUM))[f] = s;
    }
  }
#pragma unroll
  for (int it = 0; it < 10; ++it) {
    int i = tid + it * 256;
    int cs = i / 306, rem = i - cs * 306;
    int row = rem / 17, q = rem - row * 17;
    if (i < 2448) *(u32x4*)(smem + K2_LDS_U + cs * K2_COPY + row * K2_ROWB + q * 16) = ld[it];
  }
#pragma unroll
  for (int it = 0; it < 3; ++it) {
    int i = tid + it * 256;
    int cc = i >= 275, ck = i - cc * 275;
    if (i < 550) *(u32x4*)(smem + K2_LDS_K + cc * 4608 + ck * 16) = lk[it];
  }
  __syncthreads();

  const int cc = w >> 1, half = w & 1;
  f32x4 acc[8];
#pragma unroll
  for (int i = 0; i < 8; ++i) acc[i] = (f32x4){0, 0, 0, 0};
  {
    const int phi = l15 & 7, nu = l15 >> 3;
    const char* Fb = smem + K2_LDS_K + cc * 4608 + (259 - l15 + g) * 16;
    const char* Gb = smem + K2_LDS_U + cc * K2_UCH + (phi & 3) * K2_COPY + (nu + 2) * K2_ROWB + ((phi & 4) + 8 * g) * 2;
#pragma unroll
    for (int rr = 0; rr < 4; ++rr) {
      const int rho = half ? rr + 1 : rr - 3;
      const char* Fr = Fb - rho * 64;
      const char* Gr = Gb + (half ? (-K2_ROWB + (128 - 32 * rho) * 2) : (-32 * rho) * 2);
      f16x8 F[8], G[8];
#pragma unroll
      for (int q = 0; q < 8; ++q) F[q] = *(const f16x8*)(Fr - q * 512);
#pragma unroll
      for (int e = 0; e < 8; ++e) {
        u32x2 lo = *(const u32x2*)(Gr + e * 2 * K2_ROWB), hi = *(const u32x2*)(Gr + e * 2 * K2_ROWB + 8);
        u32x4 t = {lo[0], lo[1], hi[0], hi[1]};
        G[e] = __builtin_bit_cast(f16x8, t);
      }
#pragma unroll
      for (int e = 0; e < 8; ++e)
#pragma unroll
        for (int q = 0; q + e < 8; ++q)
          acc[q + e] = __builtin_amdgcn_mfma_f32_16x16x32_f16(F[q], G[e], acc[q + e], 0, 0, 0);
      if (rr == 0 && half == 0) {
        u32x2 lo = *(const u32x2*)(Gr - 2 * K2_ROWB), hi = *(const u32x2*)(Gr - 2 * K2_ROWB + 8);
        u32x4 t = {lo[0], lo[1], hi[0], hi[1]};
        f16x8 Gm = __builtin_bit_cast(f16x8, t);
        f16x8 F8 = *(const f16x8*)(Fr - 8 * 512);
#pragma unroll
        for (int i = 0; i < 7; ++i) acc[i] = __builtin_amdgcn_mfma_f32_16x16x32_f16(F[i + 1], Gm, acc[i], 0, 0, 0);
        acc[7] = __builtin_amdgcn_mfma_f32_16x16x32_f16(F8, Gm, acc[7], 0, 0, 0);
      }
    }
  }
  {
    float* yb = (float*)(smem + K2_LDS_Y + w * K2_YW);
#pragma unroll
    for (int i = 0; i < 8; ++i)
#pragma unroll
      for (int r = 0; r < 4; ++r) yb[(i * 4 + r) * 68 + l] = acc[i][r];
  }
  __syncthreads();
  {
    const int i = tid >> 5, nu = (tid >> 4) & 1, gg = (tid >> 2) & 3, r = tid & 3;
    const int off = (i * 4 + r) * 68 + 16 * gg + 8 * nu;
    const int row = tid >> 4, q = tid & 15;
#pragma unroll
    for (int c2 = 0; c2 < 2; ++c2) {
      const float* ya = (const float*)(smem + K2_LDS_Y + (2 * c2) * K2_YW) + off;
      const float* yb = (const float*)(smem + K2_LDS_Y + (2 * c2 + 1) * K2_YW) + off;
      f32x4 a0 = *(const f32x4*)ya, a1 = *(const f32x4*)(ya + 4);
      f32x4 b0 = *(const f32x4*)yb, b1 = *(const f32x4*)(yb + 4);
      u32x4 uu = *(const u32x4*)(smem + K2_LDS_U + c2 * K2_UCH + (row + 2) * K2_ROWB + q * 16);
      const float ksc = c2 ? ksc1 : ksc0, dk = c2 ? dk1 : dk0;
      float y[8];
#pragma unroll
      for (int j = 0; j < 4; ++j) { y[j] = (a0[j] + b0[j]) * ksc; y[4 + j] = (a1[j] + b1[j]) * ksc; }
      u32x4 o;
#pragma unroll
      for (int j = 0; j < 4; ++j) {
        unsigned yy = pk_bf16(y[2 * j], y[2 * j + 1]);
        unsigned ud = pk_bf16(h2f_lo(uu[j]) * dk, h2f_hi(uu[j]) * dk);
        unsigned z = pk_bf16(bflo(yy) + bflo(ud), bfhi(yy) + bfhi(ud));
        o[j] = pk_bf16(bflo(lx[c2][j]) + bflo(z), bfhi(lx[c2][j]) + bfhi(z));
      }
      *(u32x4*)(ws + WS_HT + (size_t)(2 * b + c2) * 4096 + tid * 16) = o;
    }
  }
}

#define K3_LDS_H 0
#define K3_LDS_ST 65536
#define K3_LDS_SIZE (65536 + 512)

__device__ __forceinline__ float gelu_t(float v) {
  float u = 0.7978845608028654f * (v + 0.044715f * v * v * v);
  float e = __builtin_amdgcn_exp2f(-2.885390081777927f * u);
  return v * __builtin_amdgcn_rcpf(1.f + e);
}

__global__ __launch_bounds__(512) void k3_gemm1(const u16* __restrict__ b1, char* __restrict__ ws) {
  __shared__ __attribute__((aligned(16))) char smem[K3_LDS_SIZE];
  const int b = blockIdx.x, tid = threadIdx.x, w = tid >> 6, l = tid & 63;
  const int g = l >> 4, l15 = l & 15;
  const int tb = (b & 7) * 4 + ((b >> 3) & 3), cg = b >> 5;
  const int ft0 = cg * 11 - (cg > 6 ? cg - 6 : 0);
  const int nft = cg < 6 ? 11 : 10;
  const int n2 = nft - 8;
  const int myn = w < n2 ? 2 : 1;
  const int myft = ft0 + (w < n2 ? 2 * w : n2 + w);

  u32x4 hv[8];
#pragma unroll
  for (int it = 0; it < 8; ++it) {
    int row = it * 64 + (tid >> 3), pc = tid & 7;
    int swz = ((row >> 1) & 1) | (((row >> 3) & 1) << 1);
    int lc = (((pc >> 1) ^ swz) << 1) | (pc & 1);
    hv[it] = *(const u32x4*)(ws + WS_HT + (size_t)row * 4096 + tb * 128 + lc * 16);
  }
  bf16x8 wf[2][16];
  {
    const char* wp = ws + WS_W1T + (size_t)myft * 16 * 1024 + l * 16;
#pragma unroll
    for (int ks = 0; ks < 16; ++ks) wf[0][ks] = *(const bf16x8*)(wp + ks * 1024);
    if (myn == 2) {
#pragma unroll
      for (int ks = 0; ks < 16; ++ks) wf[1][ks] = *(const bf16x8*)(wp + (16 + ks) * 1024);
    }
  }
  f32x4 cs[2], bb[2];
#pragma unroll
  for (int a = 0; a < 2; ++a) {
    int f = 16 * (myft + (a < myn ? a : 0)) + 4 * g;
    cs[a] = *(const f32x4*)(ws + WS_CSUM + f * 4);
    u32x2 bv = *(const u32x2*)(b1 + f);
    bb[a] = (f32x4){bflo(bv[0]), bfhi(bv[0]), bflo(bv[1]), bfhi(bv[1])};
  }
#pragma unroll
  for (int it = 0; it < 8; ++it) *(u32x4*)(smem + K3_LDS_H + (it * 512 + tid) * 16) = hv[it];
  __syncthreads();

  f32x4 acc[2][4];
#pragma unroll
  for (int a = 0; a < 2; ++a)
#pragma unroll
    for (int t = 0; t < 4; ++t) acc[a][t] = (f32x4){0, 0, 0, 0};
  float s1 = 0.f, s2 = 0.f;
  {
    const int q = l15 >> 2, p = l15 & 3;
    const int swz = ((q >> 1) & 1) | ((g & 1) << 1);
    const unsigned lbase = (unsigned)(size_t)(smem + K3_LDS_H) + (8 * g + q) * 128 + p * 8;
    const bf16x2 ones = {(__bf16)1.0f, (__bf16)1.0f};
#pragma unroll
    for (int ks = 0; ks < 16; ++ks) {
      bf16x8 bf[4];
#pragma unroll
      for (int t = 0; t < 4; ++t) {
        unsigned ad = lbase + ks * 4096 + ((t ^ swz) << 5);
        s16x4 lo = __builtin_amdgcn_ds_read_tr16_b64_v4i16((LDS_AS s16x4*)(size_t)ad);
        s16x4 hi = __builtin_amdgcn_ds_read_tr16_b64_v4i16((LDS_AS s16x4*)(size_t)(ad + 512));
        u32x2 l2 = __builtin_bit_cast(u32x2, lo), h2 = __builtin_bit_cast(u32x2, hi);
        u32x4 tt = {l2[0], l2[1], h2[0], h2[1]};
        bf[t] = __builtin_bit_cast(bf16x8, tt);
        if (w == 4 + t) {
#pragma unroll
          for (int j = 0; j < 4; ++j) {
            unsigned tj = tt[j];
            bf16x2 pr = __builtin_bit_cast(bf16x2, tj);
            s1 = __builtin_amdgcn_fdot2_f32_bf16(pr, ones, s1, false);
            s2 = __builtin_amdgcn_fdot2_f32_bf16(pr, pr, s2, false);
          }
        }
      }
#pragma unroll
      for (int t = 0; t < 4; ++t) acc[0][t] = __builtin_amdgcn_mfma_f32_16x16x32_bf16(wf[0][ks], bf[t], acc[0][t], 0, 0, 0);
      if (myn == 2) {
#pragma unroll
        for (int t = 0; t < 4; ++t) acc[1][t] = __builtin_amdgcn_mfma_f32_16x16x32_bf16(wf[1][ks], bf[t], acc[1][t], 0, 0, 0);
      }
    }
  }
  if (w >= 4) {
    s1 += __shfl_xor(s1, 16); s1 += __shfl_xor(s1, 32);
    s2 += __shfl_xor(s2, 16); s2 += __shfl_xor(s2, 32);
    float mean = s1 * (1.f / 512.f);
    float var = fmaxf(s2 * (1.f / 512.f) - mean * mean, 0.f);
    float m1 = rbf(mean);
    float den1 = rbf(sqrtf(rbf(rbf(var) + 1e-5f)));
    float r1 = 1.f / den1;
    float mean2 = (mean - m1) * r1, var2 = var * r1 * r1;
    float m2 = rbf(mean2);
    float den2 = rbf(sqrtf(rbf(rbf(var2) + 1e-5f)));
    float r2 = 1.f / den2;
    if (l < 16) {
      f32x2 st = {r1 * r2, (m1 * r1 + m2) * r2};
      *(f32x2*)(smem + K3_LDS_ST + ((w - 4) * 16 + l) * 8) = st;
    }
  }
  __syncthreads();
#pragma unroll
  for (int a = 0; a < 2; ++a) {
    if (a < myn) {
      const int ft = myft + a;
#pragma unroll
      for (int t = 0; t < 4; ++t) {
        f32x2 st = *(const f32x2*)(smem + K3_LDS_ST + (t * 16 + l15) * 8);
        float o[4];
#pragma unroll
        for (int r = 0; r < 4; ++r) {
          float v = rbf(st[0] * acc[a][t][r] - st[1] * cs[a][r]);
          v = rbf(v + bb[a][r]);
          o[r] = gelu_t(v);
        }
        u32x2 pk = {pk_bf16(o[0], o[1]), pk_bf16(o[2], o[3])};
        *(u32x2*)(ws + WS_HACT + ((size_t)((tb * 4 + t) * NKS2 + (ft >> 1)) * 64 + l) * 16 + (ft & 1) * 8) = pk;
      }
    }
  }
}

#define K4_LDS_SIZE (4 * 16 * 1024)

__global__ __launch_bounds__(256) void k4_gemm2(const u16* __restrict__ b2, const char* __restrict__ ws,
                                                u16* __restrict__ out) {
  __shared__ __attribute__((aligned(16))) char smem[K4_LDS_SIZE];
  const int b = blockIdx.x, tid = threadIdx.x, w = tid >> 6, l = tid & 63;
  const int g = l >> 4, l15 = l & 15;
  const int tb = (b & 7) * 4 + ((b >> 3) & 3), nb = b >> 5;
  const int ks0 = w * 11;
  const int nks = w < 3 ? 11 : 10;

  const char* ap = ws + WS_W2T + (size_t)(nb * 4) * NKS2 * 1024 + ks0 * 1024 + l * 16;
  const char* bp = ws + WS_HACT + (size_t)(tb * 4) * NKS2 * 1024 + ks0 * 1024 + l * 16;
  bf16x8 A[11][4], B[11][4];
#pragma unroll
  for (int k = 0; k < 11; ++k) {
    if (k < 10 || nks == 11) {
#pragma unroll
      for (int a = 0; a < 4; ++a) {
        A[k][a] = *(const bf16x8*)(ap + (size_t)a * NKS2 * 1024 + k * 1024);
        B[k][a] = *(const bf16x8*)(bp + (size_t)a * NKS2 * 1024 + k * 1024);
      }
    }
  }
  u16 hres[4][4];
#pragma unroll
  for (int t = 0; t < 4; ++t)
#pragma unroll
    for (int r = 0; r < 4; ++r)
      hres[t][r] = *(const u16*)(ws + WS_HT + (size_t)(nb * 64 + w * 16 + 4 * g + r) * 4096 + (tb * 64 + t * 16 + l15) * 2);
  u32x2 bv = *(const u32x2*)(b2 + nb * 64 + w * 16 + 4 * g);

  f32x4 acc[4][4];
#pragma unroll
  for (int a = 0; a < 4; ++a)
#pragma unroll
    for (int t = 0; t < 4; ++t) acc[a][t] = (f32x4){0, 0, 0, 0};
#pragma unroll
  for (int k = 0; k < 11; ++k) {
    if (k < 10 || nks == 11) {
#pragma unroll
      for (int a = 0; a < 4; ++a)
#pragma unroll
        for (int t = 0; t < 4; ++t)
          acc[a][t] = __builtin_amdgcn_mfma_f32_16x16x32_bf16(A[k][a], B[k][t], acc[a][t], 0, 0, 0);
    }
  }
#pragma unroll
  for (int a = 0; a < 4; ++a)
#pragma unroll
    for (int t = 0; t < 4; ++t) *(f32x4*)(smem + ((w * 4 + a) * 4 + t) * 1024 + l * 16) = acc[a][t];
  __syncthreads();
  {
    const float bb[4] = {bflo(bv[0]), bfhi(bv[0]), bflo(bv[1]), bfhi(bv[1])};
#pragma unroll
    for (int t = 0; t < 4; ++t) {
      f32x4 s = *(const f32x4*)(smem + ((0 * 4 + w) * 4 + t) * 1024 + l * 16);
#pragma unroll
      for (int ww = 1; ww < 4; ++ww) s += *(const f32x4*)(smem + ((ww * 4 + w) * 4 + t) * 1024 + l * 16);
      float o[4];
#pragma unroll
      for (int r = 0; r < 4; ++r) o[r] = bf2f(hres[t][r]) + rbf(rbf(s[r]) + bb[r]);
      u32x2 pk = {pk_bf16(o[0], o[1]), pk_bf16(o[2], o[3])};
      *(u32x2*)(out + (size_t)(tb * 64 + t * 16 + l15) * DM + nb * 64 + w * 16 + 4 * g) = pk;
    }
  }
}

extern "C" void kernel_launch(void* const* d_in, const int* in_sizes, int n_in,
                              void* d_out, int out_size, void* d_ws, size_t ws_size,
                              hipStream_t stream) {
  const u16* x = (const u16*)d_in[0];
  const u16* lar = (const u16*)d_in[1];
  const u16* aim = (const u16*)d_in[2];
  const u16* cr = (const u16*)d_in[3];
  const u16* ci = (const u16*)d_in[4];
  const u16* ldt = (const u16*)d_in[5];
  const u16* dsk = (const u16*)d_in[6];
  const u16* w1 = (const u16*)d_in[8];
  const u16* b1 = (const u16*)d_in[9];
  const u16* w2 = (const u16*)d_in[10];
  const u16* b2 = (const u16*)d_in[11];
  char* ws = (char*)d_ws;
  k1_prep<<<256, 256, 0, stream>>>(x, lar, aim, cr, ci, ldt, w1, w2, ws);
  k2_conv<<<256, 256, 0, stream>>>(dsk, ws);
  k3_gemm1<<<256, 512, 0, stream>>>(b1, ws);
  k4_gemm2<<<256, 256, 0, stream>>>(b2, ws, (u16*)d_out);
}
```

```cpp
#include <hip/hip_runtime.h>
#include <hip/hip_bf16.h>
#include <stdint.h>

typedef unsigned short u16;
typedef __attribute__((ext_vector_type(8))) _Float16 f16x8;
typedef __attribute__((ext_vector_type(2))) _Float16 f16x2;
typedef __attribute__((ext_vector_type(8))) __bf16 bf16x8;
typedef __attribute__((ext_vector_type(2))) __bf16 bf16x2;
typedef __attribute__((ext_vector_type(2))) float f32x2;
typedef __attribute__((ext_vector_type(4))) float f32x4;
typedef __attribute__((ext_vector_type(4))) unsigned u32x4;
typedef __attribute__((ext_vector_type(2))) unsigned u32x2;
typedef __attribute__((ext_vector_type(4))) short s16x4;
#define LDS_AS __attribute__((address_space(3)))

#define SEQ 2048
#define DM 512
#define NM 128
#define FF 1376
#define NFT 86
#define NKS2 43

#define NFTP 88
#define NKSP 44
#define UT_FRONT 512
#define UT_PITCH 4736
#define XT_PITCH 4224
#define WS_SYNC 0
#define WS_UT 8192
#define KR_PITCH 4608
#define WS_KR (WS_UT + DM * UT_PITCH)
#define WS_KSC (WS_KR + DM * KR_PITCH)
#define WS_W1T (WS_KSC + DM * 4)
#define WS_W2T (WS_W1T + NFTP * 16 * 1024)
#define WS_XT (WS_W2T + 32 * NKSP * 1024)
#define WS_HT (WS_XT + DM * XT_PITCH)
#define WS_HACT (WS_HT + DM * XT_PITCH)
#define WS_END (WS_HACT + 128 * NKSP * 1024)

#define STAMP(k, i)
#define STAMP_END(k, i)

__device__ __forceinline__ float bf2f(unsigned v) { return __uint_as_float(v << 16); }
__device__ __forceinline__ float bflo(unsigned v) { return __uint_as_float(v << 16); }
__device__ __forceinline__ float bfhi(unsigned v) { return __uint_as_float(v & 0xffff0000u); }
__device__ __forceinline__ unsigned pk_bf16(float a, float b) {
  f32x2 v = {a, b};
  bf16x2 r = __builtin_convertvector(v, bf16x2);
  return __builtin_bit_cast(unsigned, r);
}
__device__ __forceinline__ unsigned pk_f16(float a, float b) {
  f32x2 v = {a, b};
  f16x2 r = __builtin_convertvector(v, f16x2);
  return __builtin_bit_cast(unsigned, r);
}
__device__ __forceinline__ float rbf(float a) { return bflo(pk_bf16(a, a)); }
__device__ __forceinline__ float h2f_lo(unsigned v) { f16x2 h = __builtin_bit_cast(f16x2, v); return (float)h[0]; }
__device__ __forceinline__ float h2f_hi(unsigned v) { f16x2 h = __builtin_bit_cast(f16x2, v); return (float)h[1]; }
__device__ __forceinline__ unsigned lds_addr(const void* p) { return (unsigned)(size_t)p; }
__device__ __forceinline__ u32x2 tr_read(unsigned ad) {
  s16x4 v = __builtin_amdgcn_ds_read_tr16_b64_v4i16((LDS_AS s16x4*)(size_t)ad);
  return __builtin_bit_cast(u32x2, v);
}
__device__ __forceinline__ void glds16(const void* gsrc, unsigned lds_dst) {
  unsigned keep;
  asm volatile("s_mov_b32 %0, m0\n\ts_mov_b32 m0, %2\n\ts_nop 0\n\tglobal_load_lds_dwordx4 %1, off\n\ts_mov_b32 m0, %0"
               : "=&s"(keep) : "v"(gsrc), "s"(lds_dst) : "memory");
}
__device__ __forceinline__ void glds16_sc1(const void* gsrc, unsigned lds_dst) {
  unsigned keep;
  asm volatile("s_mov_b32 %0, m0\n\ts_mov_b32 m0, %2\n\ts_nop 0\n\tglobal_load_lds_dwordx4 %1, off sc1\n\ts_mov_b32 m0, %0"
               : "=&s"(keep) : "v"(gsrc), "s"(lds_dst) : "memory");
}
__device__ __forceinline__ void store8_dev(void* p, u32x2 v) {
  unsigned long long x = ((unsigned long long)v[1] << 32) | v[0];
  __hip_atomic_store((unsigned long long*)p, x, __ATOMIC_RELAXED, __HIP_MEMORY_SCOPE_AGENT);
}
__device__ __forceinline__ void raw_barrier() {
  asm volatile("s_waitcnt lgkmcnt(0)" ::: "memory");
  __builtin_amdgcn_s_barrier();
  asm volatile("" ::: "memory");
}
template <int CTRL>
__device__ __forceinline__ float dpp_add(float v) {
  int r = __builtin_amdgcn_update_dpp(0, __float_as_int(v), CTRL, 0xf, 0xf, false);
  return v + __int_as_float(r);
}
__device__ __forceinline__ float row16_sum(float v) {
  v = dpp_add<0xB1>(v);
  v = dpp_add<0x4E>(v);
  v = dpp_add<0x141>(v);
  v = dpp_add<0x140>(v);
  return v;
}

struct cpx { float r, i; };
__device__ __forceinline__ cpx cmul(cpx a, cpx b) {
  cpx o; o.r = a.r * b.r - a.i * b.i; o.i = a.r * b.i + a.i * b.r; return o;
}
__device__ __forceinline__ cpx cexp_dir(float lr2, float fr, float e) {
  float mag = __builtin_amdgcn_exp2f(lr2 * e);
  float ph = __builtin_amdgcn_fractf(fr * e);
  cpx o; o.r = mag * __builtin_amdgcn_cosf(ph); o.i = mag * __builtin_amdgcn_sinf(ph); return o;
}

#define K1_AROW 528
#define K1_IMG (32 * K1_AROW)
#define K1_LDS_A 0
#define K1_LDS_B (2 * K1_IMG)
#define K1_KCB 1344
#define K1_LDS_KC (4 * K1_IMG)
#define K1_LDS_RED (K1_LDS_KC + 2 * K1_KCB)
#define K1_LDS_W (K1_LDS_RED + 256)
#define K1_LDS_SIZE (K1_LDS_W + 4 * 2 * 2560)

__global__ __launch_bounds__(1024) void k1_prep(const u16* __restrict__ x, const u16* __restrict__ lar,
                                                const u16* __restrict__ aim, const u16* __restrict__ cre,
                                                const u16* __restrict__ cim, const u16* __restrict__ ldt,
                                                const u16* __restrict__ w1, const u16* __restrict__ w2,
                                                char* __restrict__ ws) {
  __shared__ __attribute__((aligned(16))) char smem[K1_LDS_SIZE];
  const int b = blockIdx.x, tid = threadIdx.x, l = tid & 63;
  const int w = __builtin_amdgcn_readfirstlane(tid >> 6);
  const int role = w >> 2, wr = w & 3;
  const int g = l >> 4, l15 = l & 15;
  float* red = (float*)(smem + K1_LDS_RED);
  STAMP(0, 0);

  if (role == 0) {
    const int rb = (b & 7) * 32 + (b >> 3);
    unsigned xv[8];
    float piv[8];
    const unsigned* xr = (const unsigned*)(x + rb * 8 * DM + 128 * wr + 2 * l);
#pragma unroll
    for (int r = 0; r < 8; ++r) xv[r] = xr[r * 256];
#pragma unroll
    for (int r = 0; r < 8; ++r) piv[r] = bf2f(x[(rb * 8 + r) * DM]);
    const int ch = 128 * wr + 2 * l;
    {
      u32x4 x0, x1;
#pragma unroll
      for (int q = 0; q < 4; ++q) {
        x0[q] = __builtin_amdgcn_perm(xv[2 * q + 1], xv[2 * q], 0x05040100u);
        x1[q] = __builtin_amdgcn_perm(xv[2 * q + 1], xv[2 * q], 0x07060302u);
      }
      char* xp = ws + WS_XT + (size_t)ch * XT_PITCH + rb * 16;
      *(u32x4*)xp = x0;
      *(u32x4*)(xp + XT_PITCH) = x1;
    }
    float s[16];
#pragma unroll
    for (int r = 0; r < 8; ++r) {
      float a = bflo(xv[r]) - piv[r], bb = bfhi(xv[r]) - piv[r];
      s[r] = a + bb;
      s[8 + r] = a * a + bb * bb;
    }
    float t8[8], t4[4];
#pragma unroll
    for (int i = 0; i < 8; ++i) {
      auto sw = __builtin_amdgcn_permlane32_swap(__float_as_uint(s[i]), __float_as_uint(s[8 + i]), false, false);
      t8[i] = __uint_as_float(sw[0]) + __uint_as_float(sw[1]);
    }
#pragma unroll
    for (int i = 0; i < 4; ++i) {
      auto sw = __builtin_amdgcn_permlane16_swap(__float_as_uint(t8[i]), __float_as_uint(t8[4 + i]), false, false);
      t4[i] = row16_sum(__uint_as_float(sw[0]) + __uint_as_float(sw[1]));
    }
    if (l15 == 0) *(f32x4*)(red + wr * 16 + g * 4) = (f32x4){t4[0], t4[1], t4[2], t4[3]};
    STAMP(0, 1);
    raw_barrier();
    STAMP(0, 2);
    float mb_l, rd_l;
    {
      const int r = l & 7;
      float t1 = red[r] + red[16 + r] + red[32 + r] + red[48 + r];
      float t2 = red[8 + r] + red[24 + r] + red[40 + r] + red[56 + r];
      float pv = piv[0];
#pragma unroll
      for (int k = 1; k < 8; ++k) pv = r == k ? piv[k] : pv;
      float dm = t1 * (1.f / 512.f);
      float var = fmaxf(t2 * (1.f / 512.f) - dm * dm, 0.f);
      mb_l = rbf(pv + dm);
      rd_l = __builtin_amdgcn_rcpf(rbf(__builtin_amdgcn_sqrtf(rbf(rbf(var) + 1e-5f))));
    }
    unsigned ub[8];
#pragma unroll
    for (int r = 0; r < 8; ++r) {
      const float mb = __int_as_float(__builtin_amdgcn_readlane(__float_as_int(mb_l), r));
      const float rd = __int_as_float(__builtin_amdgcn_readlane(__float_as_int(rd_l), r));
      unsigned dd = pk_bf16(bflo(xv[r]) - mb, bfhi(xv[r]) - mb);
      unsigned uu = pk_bf16(bflo(dd) * rd, bfhi(dd) * rd);
      ub[r] = pk_f16(bflo(uu), bfhi(uu));
    }
    u32x4 u0, u1;
#pragma unroll
    for (int q = 0; q < 4; ++q) {
      u0[q] = __builtin_amdgcn_perm(ub[2 * q + 1], ub[2 * q], 0x05040100u);
      u1[q] = __builtin_amdgcn_perm(ub[2 * q + 1], ub[2 * q], 0x07060302u);
    }
    char* up = ws + WS_UT + (size_t)ch * UT_PITCH + UT_FRONT + rb * 16;
    *(u32x4*)up = u0;
    *(u32x4*)(up + UT_PITCH) = u1;
    STAMP(0, 3);
    STAMP_END(0, 6);
    return;
  } else if (role == 3) {
    const int gw = b * 4 + wr;
    char* wl = smem + K1_LDS_W + wr * 5120;
    u32x4 v[2][2];
#pragma unroll
    for (int it = 0; it < 2; ++it) {
      const int bi = gw + it * 1024;
      if (bi < 688) {
        const int ks = bi / 43, fb = bi - ks * 43;
        const u16* src = w1 + (size_t)(32 * ks + (l >> 2)) * FF + 32 * fb + 8 * (l & 3);
        v[it][0] = *(const u32x4*)src;
        v[it][1] = *(const u32x4*)(src + 16 * FF);
      } else if (bi < 1376) {
        const int b2 = bi - 688, ks = b2 >> 4, nbk = b2 & 15;
        const u16* src = w2 + (size_t)(32 * ks + (l >> 2)) * DM + 32 * nbk + 8 * (l & 3);
        v[it][0] = *(const u32x4*)src;
        v[it][1] = *(const u32x4*)(src + 16 * DM);
      }
    }
    if (wr == 0 && l < 40) {
#pragma unroll
      for (int cc = 0; cc < 2; ++cc)
        *(u32x4*)(ws + WS_UT + (size_t)(2 * b + cc) * UT_PITCH + (l < 32 ? l * 16 : 4608 + (l - 32) * 16)) = (u32x4){0, 0, 0, 0};
    }
    if (wr == 2) {
      if (b < 32) *(u32x4*)(ws + WS_W1T + (size_t)(86 * 16 + b) * 1024 + l * 16) = (u32x4){0, 0, 0, 0};
      else if (b < 64) *(u32x4*)(ws + WS_W2T + (size_t)((b - 32) * NKSP + 43) * 1024 + l * 16) = (u32x4){0, 0, 0, 0};
      else if (b < 72) *(u32x4*)(ws + WS_SYNC + (b - 64) * 1024 + l * 16) = (u32x4){0, 0, 0, 0};
    }
#pragma unroll
    for (int it = 0; it < 2; ++it) {
      const int bi = gw + it * 1024;
      if (bi < 1376) {
        char* t = wl + it * 2560;
        *(u32x4*)(t + (l >> 2) * 80 + (l & 3) * 16) = v[it][0];
        *(u32x4*)(t + (16 + (l >> 2)) * 80 + (l & 3) * 16) = v[it][1];
        const bool isw1 = bi < 688;
        const int q = l15 >> 2, p = l15 & 3;
        const unsigned a0 = lds_addr(t) + (isw1 ? (8 * g + q) : (4 * g + q)) * 80 + p * 8;
        const unsigned dr = isw1 ? 4 * 80 : 16 * 80;
#pragma unroll
        for (int ct = 0; ct < 2; ++ct) {
          u32x2 lo = tr_read(a0 + ct * 32), hi = tr_read(a0 + ct * 32 + dr);
          u32x4 o = {lo[0], lo[1], hi[0], hi[1]};
          if (isw1) {
            const int ks = bi / 43, fb = bi - ks * 43, ft = 2 * fb + ct;
            *(u32x4*)(ws + WS_W1T + (size_t)(ft * 16 + ks) * 1024 + l * 16) = o;
          } else {
            const int b2 = bi - 688, ks = b2 >> 4, nt = 2 * (b2 & 15) + ct;
            *(u32x4*)(ws + WS_W2T + (size_t)(nt * NKSP + ks) * 1024 + l * 16) = o;
          }
        }
      }
    }
    STAMP(0, 3);
    STAMP_END(0, 6);
    return;
  } else {
    const int c = role - 1, part = wr, d = 2 * b + c;
    const unsigned v_lar = *(const unsigned*)(lar + d * NM + 2 * l), v_aim = *(const unsigned*)(aim + d * NM + 2 * l);
    const unsigned v_cr = *(const unsigned*)(cre + d * NM + 2 * l), v_ci = *(const unsigned*)(cim + d * NM + 2 * l);
    const float dt = rbf(__expf(bf2f(ldt[d])));
    float lr2[2], fr[2];
    lr2[0] = (-rbf(__expf(bflo(v_lar))) * dt) * 1.4426950408889634f;
    lr2[1] = (-rbf(__expf(bfhi(v_lar))) * dt) * 1.4426950408889634f;
    fr[0] = (bflo(v_aim) * dt) * 0.15915494309189535f;
    fr[1] = (bfhi(v_aim) * dt) * 0.15915494309189535f;
    if (part == 3 && l < 2) *(u32x4*)(smem + K1_LDS_KC + c * K1_KCB + l * 16) = (u32x4){0, 0, 0, 0};
    if (part < 2) {
      float cm = fmaxf(fmaxf(fabsf(bflo(v_cr)), fabsf(bfhi(v_cr))), fmaxf(fabsf(bflo(v_ci)), fabsf(bfhi(v_ci))));
#pragma unroll
      for (int o = 32; o > 0; o >>= 1) cm = fmaxf(cm, __shfl_xor(cm, o));
      const float cinv = cm > 0.f ? 1.f / cm : 0.f;
      char* Aimg = smem + K1_LDS_A + c * K1_IMG + (16 * part) * K1_AROW + l * 8;
      cpx P[2][2], z8[2];
      const float e0 = 128.f * part;
#pragma unroll
      for (int m = 0; m < 2; ++m) {
        cpx C; C.r = (m ? bfhi(v_cr) : bflo(v_cr)) * cinv; C.i = (m ? bfhi(v_ci) : bflo(v_ci)) * cinv;
        z8[m] = cexp_dir(lr2[m], fr[m], 8.f);
        P[m][0] = cmul(C, cexp_dir(lr2[m], fr[m], e0));
        P[m][1] = cmul(C, cexp_dir(lr2[m], fr[m], e0 + 64.f));
      }
#pragma unroll
      for (int a = 0; a < 8; ++a) {
#pragma unroll
        for (int k = 0; k < 2; ++k) {
          u32x2 o = {pk_f16(P[0][k].r, P[0][k].i), pk_f16(P[1][k].r, P[1][k].i)};
          *(u32x2*)(Aimg + (8 * k + a) * K1_AROW) = o;
          if (a < 7) { P[0][k] = cmul(P[0][k], z8[0]); P[1][k] = cmul(P[1][k], z8[1]); }
        }
      }
      if (part == 0 && l == 0) ((float*)(ws + WS_KSC))[d] = 2.f * dt * cm * 128.f;
    } else {
      char* Bimg = smem + K1_LDS_B + c * K1_IMG + l * 8;
#define PUTB(col, v0, v1) { cpx _a = (v0), _b = (v1); u32x2 _o = {pk_f16(_a.r, -_a.i), pk_f16(_b.r, -_b.i)}; *(u32x2*)(Bimg + (col) * K1_AROW) = _o; }
      if (part == 2) {
        cpx z[2][8], e256[2];
#pragma unroll
        for (int m = 0; m < 2; ++m) {
          z[m][0].r = 1.f; z[m][0].i = 0.f;
          z[m][1] = cexp_dir(lr2[m], fr[m], 1.f);
          z[m][2] = cmul(z[m][1], z[m][1]); z[m][3] = cmul(z[m][2], z[m][1]); z[m][4] = cmul(z[m][2], z[m][2]);
          z[m][5] = cmul(z[m][4], z[m][1]); z[m][6] = cmul(z[m][4], z[m][2]); z[m][7] = cmul(z[m][4], z[m][3]);
          e256[m] = cexp_dir(lr2[m], fr[m], 256.f);
        }
#pragma unroll
        for (int k = 0; k < 8; ++k) PUTB(k, z[0][k], z[1][k]);
        PUTB(8, e256[0], e256[1]);
#pragma unroll
        for (int k = 1; k < 4; ++k) PUTB(8 + k, cmul(e256[0], z[0][2 * k]), cmul(e256[1], z[1][2 * k]));
      } else {
        cpx z4[2], e256[2], e512[2], e768[2], e1024[2];
#pragma unroll
        for (int m = 0; m < 2; ++m) {
          z4[m] = cexp_dir(lr2[m], fr[m], 4.f);
          e256[m] = cexp_dir(lr2[m], fr[m], 256.f);
          e1024[m] = cexp_dir(lr2[m], fr[m], 1024.f);
          e512[m] = cmul(e256[m], e256[m]); e768[m] = cmul(e512[m], e256[m]);
        }
        PUTB(12, e512[0], e512[1]); PUTB(13, cmul(e512[0], z4[0]), cmul(e512[1], z4[1]));
        PUTB(14, e768[0], e768[1]); PUTB(15, cmul(e768[0], z4[0]), cmul(e768[1], z4[1]));
        PUTB(16, e1024[0], e1024[1]);
        PUTB(17, cmul(e1024[0], e256[0]), cmul(e1024[1], e256[1]));
        PUTB(18, cmul(e1024[0], e512[0]), cmul(e1024[1], e512[1]));
        PUTB(19, cmul(e1024[0], e768[0]), cmul(e1024[1], e768[1]));
        PUTB(20, cmul(e1024[0], e1024[0]), cmul(e1024[1], e1024[1]));
      }
#undef PUTB
    }
    STAMP(0, 1);
    raw_barrier();
    STAMP(0, 2);
    {
      const int rt = wr >> 1, ct = wr & 1;
      const char* Ab = smem + K1_LDS_A + c * K1_IMG + (16 * rt + l15) * K1_AROW + g * 16;
      const int bc = 16 * ct + l15;
      const char* Bb = smem + K1_LDS_B + c * K1_IMG + (bc < 21 ? bc : 20) * K1_AROW + g * 16;
      f16x8 af[8], bfr[8];
#pragma unroll
      for (int q = 0; q < 8; ++q) { af[q] = *(const f16x8*)(Ab + q * 64); bfr[q] = *(const f16x8*)(Bb + q * 64); }
      f32x4 acc = {0, 0, 0, 0};
#pragma unroll
      for (int q = 0; q < 8; ++q) acc = __builtin_amdgcn_mfma_f32_16x16x32_f16(af[q], bfr[q], acc, 0, 0, 0);
      _Float16* kc = (_Float16*)(smem + K1_LDS_KC + c * K1_KCB + 32);
      const int a0 = 16 * rt + 4 * g;
      int base, stride;
      if (ct == 0) {
        if (l15 < 8) { base = l15; stride = 8; }
        else if (l15 < 12) { base = 256 + (l15 - 8); stride = 4; }
        else { base = 384 + 64 * ((l15 - 12) >> 1) + (l15 & 1); stride = 2; }
      } else {
        base = 512 + 32 * l15; stride = 1;
      }
#pragma unroll
      for (int r = 0; r < 4; ++r) {
        const bool ok = ct == 0 || l15 < 4 || (l15 == 4 && a0 + r == 0);
        if (ok) kc[base + stride * (a0 + r)] = (_Float16)(acc[r] * (1.f / 128.f));
      }
    }
    STAMP(0, 3);
  }
  raw_barrier();
  STAMP(0, 4);
#pragma unroll
  for (int it = 0; it < 2; ++it) {
    const int ei = tid - 256 + it * 512;
    if (ei >= 550) break;
    const int cc = ei >= 275, ck = ei - cc * 275;
    int c2 = 259 - ck;
    const bool top = c2 == 256;
    if (c2 > 256 || c2 < 0) c2 = -1;
    int ih; unsigned tbl;
    if (c2 <= 32) { ih = 8 * c2; tbl = 0x76543210u; }
    else if (c2 <= 64) { ih = 256 + 4 * (c2 - 32); tbl = 0x43222100u; }
    else if (c2 <= 128) { ih = 384 + 2 * (c2 - 64); tbl = 0x22111000u; }
    else { ih = 512 + (c2 - 128); tbl = 0x11100000u | ((unsigned)(c2 & 1) << 16); }
    const u16* kc = (const u16*)(smem + K1_LDS_KC + cc * K1_KCB + 32) + ih;
    unsigned v[8];
#pragma unroll
    for (int j = 0; j < 8; ++j) v[j] = kc[-(int)((tbl >> (4 * j)) & 15u)];
    if (top) v[0] = 0u;
    u32x4 o = {v[0] | (v[1] << 16), v[2] | (v[3] << 16), v[4] | (v[5] << 16), v[6] | (v[7] << 16)};
    *(u32x4*)(ws + WS_KR + (size_t)(2 * b + cc) * KR_PITCH + ck * 16) = o;
  }
  STAMP(0, 5);
  STAMP_END(0, 6);
}

#define K2_ROWB 272
#define K2_COPY (18 * K2_ROWB)
#define K2_UCH (4 * K2_COPY)
#define K2_LDS_U 0
#define K2_LDS_K (39 * 1024)
#define K2_LDS_X (K2_LDS_K + 2 * 4608)
#define K2_LDS_Y (K2_LDS_X + 8192)
#define K2_LDS_END (K2_LDS_Y + 16384)
#define K3_LDS_H 0
#define K3_LDS_ST 65536
#define K4_LDS_R 0
#define KB_LDS_SIZE K2_LDS_END
#define SPIN_MAX (1u << 22)

__device__ __forceinline__ f16x8 ld_g(const char* p) {
  u32x2 lo = *(const u32x2*)p, hi = *(const u32x2*)(p + 8);
  u32x4 t = {lo[0], lo[1], hi[0], hi[1]};
  return __builtin_bit_cast(f16x8, t);
}
__device__ __forceinline__ float gelu_t(float v) {
  float p = fmaf(v * v, -0.10294324f, -2.30220819f);
  float e = __builtin_amdgcn_exp2f(v * p);
  return v * __builtin_amdgcn_rcpf(1.f + e);
}
__device__ __forceinline__ bf16x8 ld_bfrag(unsigned ad) {
  u32x2 l2 = tr_read(ad), h2 = tr_read(ad + 512);
  u32x4 tt = {l2[0], l2[1], h2[0], h2[1]};
  return __builtin_bit_cast(bf16x8, tt);
}

template <int half>
__device__ __forceinline__ void conv_body(char* __restrict__ ws, char* smem, int cp, int w, int l, float ksc, float dk,
                                          bf16x8 (&wf)[3][16], const char* wp) {
  const int g = l >> 4, l15 = l & 15;
  const int cc = w >> 1;
  f32x4 acc[8];
#pragma unroll
  for (int i = 0; i < 8; ++i) acc[i] = (f32x4){0, 0, 0, 0};
  const int phi = l15 & 7, nu = l15 >> 3;
  const char* Fb = smem + K2_LDS_K + cc * 4608 + (259 - l15 + g) * 16 + 192 - 256 * half;
  const char* Gb = smem + K2_LDS_U + cc * K2_UCH + (phi & 3) * K2_COPY + (nu + 2) * K2_ROWB + ((phi & 4) + 8 * g) * 2 + 192 - half * K2_ROWB;
  f16x8 F[2][8], G[2][8];
#pragma unroll
  for (int q = 0; q < 8; ++q) { G[0][q] = ld_g(Gb + q * 2 * K2_ROWB); F[0][q] = *(const f16x8*)(Fb - q * 512); }
  f16x8 Gm, F8;
  if (half == 0) { Gm = ld_g(Gb - 2 * K2_ROWB); F8 = *(const f16x8*)(Fb - 8 * 512); }
  __builtin_amdgcn_sched_barrier(0);
#pragma unroll
  for (int rr = 0; rr < 4; ++rr) {
    const int cur = rr & 1, nxt = cur ^ 1;
    if (rr < 3) {
#pragma unroll
      for (int q = 0; q < 8; ++q) {
        G[nxt][q] = ld_g(Gb - (rr + 1) * 64 + q * 2 * K2_ROWB);
        F[nxt][q] = *(const f16x8*)(Fb - (rr + 1) * 64 - q * 512);
      }
    }
#pragma unroll
    for (int k4 = 0; k4 < 4; ++k4) wf[0][rr * 4 + k4] = *(const bf16x8*)(wp + (rr * 4 + k4) * 1024);
#pragma unroll
    for (int e = 0; e < 8; ++e)
#pragma unroll
      for (int q = 0; q + e < 8; ++q)
        acc[q + e] = __builtin_amdgcn_mfma_f32_16x16x32_f16(G[cur][e], F[cur][q], acc[q + e], 0, 0, 0);
    if (rr == 0 && half == 0) {
#pragma unroll
      for (int i = 0; i < 7; ++i) acc[i] = __builtin_amdgcn_mfma_f32_16x16x32_f16(Gm, F[0][i + 1], acc[i], 0, 0, 0);
      acc[7] = __builtin_amdgcn_mfma_f32_16x16x32_f16(Gm, F8, acc[7], 0, 0, 0);
    }
#pragma unroll
    for (int k = 0; k < 4; ++k) {
#pragma unroll
      for (int j = 0; j < 4; ++j) {
        __builtin_amdgcn_sched_group_barrier(0x008, 2, 0);
        if (rr < 3) __builtin_amdgcn_sched_group_barrier(0x100, 1, 0);
      }
      __builtin_amdgcn_sched_group_barrier(0x008, 1, 0);
      __builtin_amdgcn_sched_group_barrier(0x020, 1, 0);
    }
    __builtin_amdgcn_sched_barrier(0);
  }
  STAMP(1, 2);
#pragma unroll
  for (int i = 0; i < 4; ++i)
    *(f32x4*)(smem + K2_LDS_Y + (w * 4 + i) * 1024 + l * 16) = half ? acc[i] : acc[4 + i];
  raw_barrier();
  const int tl = 128 * (g >> 1) + 8 * l15 + 4 * (g & 1);
#pragma unroll
  for (int i = 0; i < 4; ++i) {
    const int ti = 4 * half + i;
    f32x4 o = *(const f32x4*)(smem + K2_LDS_Y + ((w ^ 1) * 4 + i) * 1024 + l * 16);
    o += half ? acc[4 + i] : acc[i];
    const int t0 = 256 * ti + tl;
    u32x2 uu = *(const u32x2*)(smem + K2_LDS_U + cc * K2_UCH + ((t0 >> 7) + 2) * K2_ROWB + (t0 & 127) * 2);
    u32x2 xx = *(const u32x2*)(smem + K2_LDS_X + cc * 4096 + t0 * 2);
    u32x2 res;
#pragma unroll
    for (int j = 0; j < 2; ++j) {
      unsigned yy = pk_bf16(o[2 * j] * ksc, o[2 * j + 1] * ksc);
      unsigned ud = pk_bf16(h2f_lo(uu[j]) * dk, h2f_hi(uu[j]) * dk);
      unsigned z = pk_bf16(bflo(yy) + bflo(ud), bfhi(yy) + bfhi(ud));
      res[j] = pk_bf16(bflo(xx[j]) + bflo(z), bfhi(xx[j]) + bfhi(z));
    }
    store8_dev(ws + WS_HT + (size_t)(2 * cp + cc) * XT_PITCH + t0 * 2, res);
  }
}

__device__ __forceinline__ u32x2 gelu4(const f32x4& acc, const f32x2& st, const f32x4& cs, const f32x4& bb) {
  float o[4];
#pragma unroll
  for (int r = 0; r < 4; ++r) o[r] = gelu_t(fmaf(st[0], acc[r], fmaf(st[1], cs[r], bb[r])));
  return (u32x2){pk_bf16(o[0], o[1]), pk_bf16(o[2], o[3])};
}

__global__ __launch_bounds__(256) void kb_main(const u16* __restrict__ dsk,
                                               const u16* __restrict__ b1, const u16* __restrict__ b2,
                                               char* __restrict__ ws, u16* __restrict__ out) {
  __shared__ __attribute__((aligned(16))) char smem[KB_LDS_SIZE];
  const int b = blockIdx.x, tid = threadIdx.x, l = tid & 63;
  const int w = __builtin_amdgcn_readfirstlane(tid >> 6);
  const int g = l >> 4, l15 = l & 15;
  const int cp = b;
  const int tb = (b & 7) * 4 + ((b >> 3) & 3);
  const int cg = b >> 5;
  const unsigned sb = lds_addr(smem);
  unsigned* sync = (unsigned*)(ws + WS_SYNC);
  STAMP(1, 0);
#pragma unroll
  for (int it = 0; it < 14; ++it) {
    const int j = w + it * 4;
    if (j < 39) {
      int ci = j * 64 + l;
      ci = ci < 2448 ? ci : 2447;
      const int cs = ci / 306, rem = ci - cs * 306;
      const int row = rem / 17, q = rem - row * 17;
      glds16(ws + WS_UT + (size_t)(2 * cp + (cs >> 2)) * UT_PITCH + (128 * row + 8 * q + (cs & 3)) * 2, sb + K2_LDS_U + j * 1024);
    } else if (j < 48) {
      const int jk = j - 39;
      glds16(ws + WS_KR + (size_t)(2 * cp) * KR_PITCH + jk * 1024 + l * 16, sb + K2_LDS_K + jk * 1024);
    } else {
      const int jx = j - 48;
      glds16(ws + WS_XT + (size_t)(2 * cp + (jx >> 2)) * XT_PITCH + (jx & 3) * 1024 + l * 16, sb + K2_LDS_X + jx * 1024);
    }
  }
  const float ksc = ((const float*)(ws + WS_KSC))[2 * cp + (w >> 1)];
  unsigned dkv = *(const unsigned*)(dsk + 2 * cp);
  const int myft = cg * 11 + 3 * w;
  bf16x8 wf[3][16];
  const char* wp = ws + WS_W1T + (size_t)myft * 16 * 1024 + l * 16;
  asm volatile("" :: "v"(dkv), "v"(ksc) : "memory");
  raw_barrier();
  STAMP(1, 1);
  {
    const float dk = (w >> 1) ? bfhi(dkv) : bflo(dkv);
    if (w & 1) conv_body<1>(ws, smem, cp, w, l, ksc, dk, wf, wp);
    else conv_body<0>(ws, smem, cp, w, l, ksc, dk, wf, wp);
  }
  STAMP(1, 3);
  asm volatile("s_waitcnt vmcnt(0)" ::: "memory");
  raw_barrier();
  if (tid == 0) __hip_atomic_fetch_add(sync + (b & 7) * 32, 1u, __ATOMIC_RELAXED, __HIP_MEMORY_SCOPE_AGENT);
  STAMP(1, 4);
  {
    const int a2 = w < 3 ? 2 : 1;
#pragma unroll
    for (int ks = 0; ks < 16; ++ks) {
      wf[1][ks] = *(const bf16x8*)(wp + (16 + ks) * 1024);
      wf[2][ks] = *(const bf16x8*)(wp + (16 * a2 + ks) * 1024);
    }
  }
  f32x4 bb[3];
#pragma unroll
  for (int a = 0; a < 3; ++a) {
    int f = 16 * (myft + a) + 4 * g;
    u32x2 bv = {0u, 0u};
    if (f < FF) bv = *(const u32x2*)(b1 + f);
    bb[a] = (f32x4){bflo(bv[0]), bfhi(bv[0]), bflo(bv[1]), bfhi(bv[1])};
  }
  const u32x2 b2v = *(const u32x2*)(b2 + cg * 64 + w * 16 + 4 * g);
  __builtin_amdgcn_sched_barrier(0);
  f32x4 cs[3];
  {
    const bf16x2 ones = {(__bf16)1.0f, (__bf16)1.0f};
#pragma unroll
    for (int a = 0; a < 3; ++a) {
      float s0 = 0.f, s1 = 0.f;
#pragma unroll
      for (int ks = 0; ks < 16; ++ks) {
        u32x4 t = __builtin_bit_cast(u32x4, wf[a][ks]);
        unsigned t0 = t[0], t1 = t[1], t2 = t[2], t3 = t[3];
        s0 = __builtin_amdgcn_fdot2_f32_bf16(__builtin_bit_cast(bf16x2, t0), ones, s0, false);
        s1 = __builtin_amdgcn_fdot2_f32_bf16(__builtin_bit_cast(bf16x2, t1), ones, s1, false);
        s0 = __builtin_amdgcn_fdot2_f32_bf16(__builtin_bit_cast(bf16x2, t2), ones, s0, false);
        s1 = __builtin_amdgcn_fdot2_f32_bf16(__builtin_bit_cast(bf16x2, t3), ones, s1, false);
      }
      float sm = s0 + s1;
      sm += __shfl_xor(sm, 16);
      sm += __shfl_xor(sm, 32);
#pragma unroll
      for (int r = 0; r < 4; ++r) cs[a][r] = __shfl(sm, 4 * g + r);
    }
  }
  STAMP(1, 5);
  if (w == 0) {
#pragma unroll 1
    for (unsigned it = 0; it < SPIN_MAX; ++it) {
      unsigned v = __hip_atomic_load(sync + (l & 7) * 32, __ATOMIC_RELAXED, __HIP_MEMORY_SCOPE_AGENT);
      if (__all(v >= 32u)) break;
      __builtin_amdgcn_s_sleep(1);
    }
  }
  raw_barrier();
  STAMP(2, 0);
#pragma unroll
  for (int it = 0; it < 16; ++it) {
    const int j = it * 4 + w;
    const int row = j * 8 + (l >> 3), pc = l & 7;
    const int swz = ((row >> 1) & 1) | (((row >> 3) & 1) << 1);
    const int lc = (((pc >> 1) ^ swz) << 1) | (pc & 1);
    glds16_sc1(ws + WS_HT + (size_t)row * XT_PITCH + tb * 128 + lc * 16, sb + K3_LDS_H + j * 1024);
  }
  asm volatile("s_waitcnt vmcnt(0)" ::: "memory");
  raw_barrier();
  STAMP(2, 1);
  bf16x8 w2f[11][4];
  const char* w2p = ws + WS_W2T + (size_t)(cg * 4) * NKSP * 1024 + (w * 11) * 1024 + l * 16;
  const int q4 = l15 >> 2, p4 = l15 & 3;
  __amdgpu_buffer_rsrc_t rs = __builtin_amdgcn_make_buffer_rsrc(ws + WS_HACT, 0, 64 * NFTP * 1024, 0x00020000);
  {
    const int swz = ((q4 >> 1) & 1) | ((g & 1) << 1);
    const unsigned lbase = sb + K3_LDS_H + (8 * g + q4) * 128 + p4 * 8;
    unsigned ad[4];
#pragma unroll
    for (int t = 0; t < 4; ++t) ad[t] = lbase + ((t ^ swz) << 5);
    const unsigned sa = lbase + ((w ^ swz) << 5);
    const bf16x2 ones = {(__bf16)1.0f, (__bf16)1.0f};
    float s1a = 0.f, s2a = 0.f, s1b = 0.f, s2b = 0.f;
    f32x4 acc0[3][2], acc1[3][2];
#pragma unroll
    for (int a = 0; a < 3; ++a)
#pragma unroll
      for (int t = 0; t < 2; ++t) { acc0[a][t] = (f32x4){0, 0, 0, 0}; acc1[a][t] = (f32x4){0, 0, 0, 0}; }
    bf16x8 bf[2][2], sf[2];
    bf[0][0] = ld_bfrag(ad[0]); bf[0][1] = ld_bfrag(ad[1]); sf[0] = ld_bfrag(sa);
    __builtin_amdgcn_sched_barrier(0);
#pragma unroll
    for (int ks = 0; ks < 16; ++ks) {
      const int cur = ks & 1;
      if (ks < 15) {
        bf[cur ^ 1][0] = ld_bfrag(ad[0] + (ks + 1) * 4096); bf[cur ^ 1][1] = ld_bfrag(ad[1] + (ks + 1) * 4096);
        sf[cur ^ 1] = ld_bfrag(sa + (ks + 1) * 4096);
      } else {
        bf[cur ^ 1][0] = ld_bfrag(ad[2]); bf[cur ^ 1][1] = ld_bfrag(ad[3]);
      }
      w2f[ks >> 2][ks & 3] = *(const bf16x8*)(w2p + (size_t)(ks & 3) * NKSP * 1024 + (ks >> 2) * 1024);
#pragma unroll
      for (int t = 0; t < 2; ++t)
#pragma unroll
        for (int a = 0; a < 3; ++a) acc0[a][t] = __builtin_amdgcn_mfma_f32_16x16x32_bf16(wf[a][ks], bf[cur][t], acc0[a][t], 0, 0, 0);
      {
        u32x4 t = __builtin_bit_cast(u32x4, sf[cur]);
        unsigned t0 = t[0], t1 = t[1], t2 = t[2], t3 = t[3];
        bf16x2 p0 = __builtin_bit_cast(bf16x2, t0), p1 = __builtin_bit_cast(bf16x2, t1);
        bf16x2 p2 = __builtin_bit_cast(bf16x2, t2), p3 = __builtin_bit_cast(bf16x2, t3);
        s1a = __builtin_amdgcn_fdot2_f32_bf16(p0, ones, s1a, false); s2a = __builtin_amdgcn_fdot2_f32_bf16(p0, p0, s2a, false);
        s1b = __builtin_amdgcn_fdot2_f32_bf16(p1, ones, s1b, false); s2b = __builtin_amdgcn_fdot2_f32_bf16(p1, p1, s2b, false);
        s1a = __builtin_amdgcn_fdot2_f32_bf16(p2, ones, s1a, false); s2a = __builtin_amdgcn_fdot2_f32_bf16(p2, p2, s2a, false);
        s1b = __builtin_amdgcn_fdot2_f32_bf16(p3, ones, s1b, false); s2b = __builtin_amdgcn_fdot2_f32_bf16(p3, p3, s2b, false);
      }
#pragma unroll
      for (int k = 0; k < 6; ++k) {
        __builtin_amdgcn_sched_group_barrier(0x008, 1, 0);
        __builtin_amdgcn_sched_group_barrier(0x100, 1, 0);
        __builtin_amdgcn_sched_group_barrier(0x002, 2, 0);
        if (k == 0) __builtin_amdgcn_sched_group_barrier(0x020, 1, 0);
      }
      __builtin_amdgcn_sched_barrier(0);
    }
    {
      float s1 = s1a + s1b, s2 = s2a + s2b;
      s1 += __shfl_xor(s1, 16); s2 += __shfl_xor(s2, 16);
      s1 += __shfl_xor(s1, 32); s2 += __shfl_xor(s2, 32);
      float mean = s1 * (1.f / 512.f);
      float var = fmaxf(s2 * (1.f / 512.f) - mean * mean, 0.f);
      float m1 = rbf(mean);
      float r1 = __builtin_amdgcn_rcpf(rbf(__builtin_amdgcn_sqrtf(rbf(rbf(var) + 1e-5f))));
      float mean2 = (mean - m1) * r1, var2 = var * r1 * r1;
      float m2 = rbf(mean2);
      float r2 = __builtin_amdgcn_rcpf(rbf(__builtin_amdgcn_sqrtf(rbf(rbf(var2) + 1e-5f))));
      if (l < 16) *(f32x2*)(smem + K3_LDS_ST + (w * 16 + l) * 8) = (f32x2){r1 * r2, -(m1 * r1 + m2) * r2};
    }
    raw_barrier();
    STAMP(2, 2);
    f32x2 st[4];
#pragma unroll
    for (int t = 0; t < 4; ++t) st[t] = *(const f32x2*)(smem + K3_LDS_ST + (t * 16 + l15) * 8);
    const unsigned so = ((tb * 2) * NFTP + myft) * 1024 + l * 16;
    u32x2 g0[3][2];
    __builtin_amdgcn_sched_barrier(0);
#pragma unroll
    for (int ks = 0; ks < 16; ++ks) {
      const int cur = ks & 1;
      if (ks < 15) { bf[cur ^ 1][0] = ld_bfrag(ad[2] + (ks + 1) * 4096); bf[cur ^ 1][1] = ld_bfrag(ad[3] + (ks + 1) * 4096); }
      w2f[4 + (ks >> 2)][ks & 3] = *(const bf16x8*)(w2p + (size_t)(ks & 3) * NKSP * 1024 + (4 + (ks >> 2)) * 1024);
#pragma unroll
      for (int t = 0; t < 2; ++t)
#pragma unroll
        for (int a = 0; a < 3; ++a) acc1[a][t] = __builtin_amdgcn_mfma_f32_16x16x32_bf16(wf[a][ks], bf[cur][t], acc1[a][t], 0, 0, 0);
      if ((ks & 1) == 0 && ks < 12) {
        const int a = ks >> 2, t = (ks >> 1) & 1;
        g0[a][t] = gelu4(acc0[a][t], st[t], cs[a], bb[a]);
      }
      if (ks == 3 || ks == 7) {
        const int a = ks >> 2;
        u32x4 v = {g0[a][0][0], g0[a][0][1], g0[a][1][0], g0[a][1][1]};
        __builtin_amdgcn_raw_buffer_store_b128(v, rs, so + a * 1024, 0, 16);
      }
#pragma unroll
      for (int k = 0; k < 6; ++k) {
        __builtin_amdgcn_sched_group_barrier(0x008, 1, 0);
        if (k < 4) __builtin_amdgcn_sched_group_barrier(0x100, 1, 0);
        __builtin_amdgcn_sched_group_barrier(0x402, 8, 0);
        if (k == 0) __builtin_amdgcn_sched_group_barrier(0x020, 1, 0);
      }
      __builtin_amdgcn_sched_barrier(0);
    }
    STAMP(2, 3);
#pragma unroll
    for (int k = 8; k < 11; ++k)
#pragma unroll
      for (int a = 0; a < 4; ++a) w2f[k][a] = *(const bf16x8*)(w2p + (size_t)a * NKSP * 1024 + k * 1024);
    __builtin_amdgcn_sched_barrier(0);
    if (w < 3) {
      u32x4 v = {g0[2][0][0], g0[2][0][1], g0[2][1][0], g0[2][1][1]};
      __builtin_amdgcn_raw_buffer_store_b128(v, rs, so + 2 * 1024, 0, 16);
    }
#pragma unroll
    for (int a = 0; a < 3; ++a) {
      if (a < 2 || w < 3) {
        u32x2 p0 = gelu4(acc1[a][0], st[2], cs[a], bb[a]), p1 = gelu4(acc1[a][1], st[3], cs[a], bb[a]);
        u32x4 v = {p0[0], p0[1], p1[0], p1[1]};
        __builtin_amdgcn_raw_buffer_store_b128(v, rs, so + (NFTP + a) * 1024, 0, 16);
      }
    }
  }
  u32x2 hres[4];
  {
    const int row = cg * 64 + w * 16 + 4 * g + q4;
    const int swz = ((row >> 1) & 1) | (((row >> 3) & 1) << 1);
    const unsigned hb = sb + K3_LDS_H + row * 128 + p4 * 8;
#pragma unroll
    for (int t = 0; t < 4; ++t) hres[t] = tr_read(hb + ((t ^ swz) << 5));
  }
  STAMP(2, 4);
  asm volatile("s_waitcnt vmcnt(0)" ::: "memory");
  raw_barrier();
  STAMP(2, 5);
  if (w == 0) {
    unsigned* pc = sync + 256 + tb * 32;
    if (l == 0) __hip_atomic_fetch_add(pc, 1u, __ATOMIC_RELAXED, __HIP_MEMORY_SCOPE_AGENT);
#pragma unroll 1
    for (unsigned it = 0; it < SPIN_MAX; ++it) {
      unsigned v = __hip_atomic_load(pc, __ATOMIC_RELAXED, __HIP_MEMORY_SCOPE_AGENT);
      if (v >= 8u) break;
      __builtin_amdgcn_s_sleep(1);
    }
  }
  raw_barrier();
  STAMP(3, 0);
  {
    const unsigned voff = ((tb * 2) * NFTP + w * 22) * 1024 + l * 16;
    u32x4 hv[11][2][2];
#pragma unroll
    for (int k = 0; k < 11; ++k)
#pragma unroll
      for (int p = 0; p < 2; ++p)
#pragma unroll
        for (int f = 0; f < 2; ++f)
          hv[k][p][f] = __builtin_amdgcn_raw_buffer_load_b128(rs, voff + (p * NFTP + 2 * k + f) * 1024, 0, 16);
    __builtin_amdgcn_sched_barrier(0);
    f32x4 acc[4][4];
#pragma unroll
    for (int a = 0; a < 4; ++a)
#pragma unroll
      for (int t = 0; t < 4; ++t) acc[a][t] = (f32x4){0, 0, 0, 0};
#pragma unroll
    for (int k = 0; k < 11; ++k)
#pragma unroll
      for (int p = 0; p < 2; ++p) {
        u32x4 x0 = {hv[k][p][0][0], hv[k][p][0][1], hv[k][p][1][0], hv[k][p][1][1]};
        u32x4 x1 = {hv[k][p][0][2], hv[k][p][0][3], hv[k][p][1][2], hv[k][p][1][3]};
        bf16x8 h0 = __builtin_bit_cast(bf16x8, x0), h1 = __builtin_bit_cast(bf16x8, x1);
#pragma unroll
        for (int a = 0; a < 4; ++a) {
          acc[a][2 * p] = __builtin_amdgcn_mfma_f32_16x16x32_bf16(w2f[k][a], h0, acc[a][2 * p], 0, 0, 0);
          acc[a][2 * p + 1] = __builtin_amdgcn_mfma_f32_16x16x32_bf16(w2f[k][a], h1, acc[a][2 * p + 1], 0, 0, 0);
        }
      }
    STAMP(3, 1);
#pragma unroll
    for (int a = 0; a < 4; ++a)
#pragma unroll
      for (int t = 0; t < 4; ++t) *(f32x4*)(smem + K4_LDS_R + ((w * 4 + a) * 4 + t) * 1024 + l * 16) = acc[a][t];
    raw_barrier();
    STAMP(3, 2);
    const float bq[4] = {bflo(b2v[0]), bfhi(b2v[0]), bflo(b2v[1]), bfhi(b2v[1])};
#pragma unroll
    for (int t = 0; t < 4; ++t) {
      f32x4 sacc = *(const f32x4*)(smem + K4_LDS_R + ((0 * 4 + w) * 4 + t) * 1024 + l * 16);
#pragma unroll
      for (int ww = 1; ww < 4; ++ww) sacc += *(const f32x4*)(smem + K4_LDS_R + ((ww * 4 + w) * 4 + t) * 1024 + l * 16);
      float o[4];
      o[0] = bflo(hres[t][0]) + rbf(rbf(sacc[0]) + bq[0]);
      o[1] = bfhi(hres[t][0]) + rbf(rbf(sacc[1]) + bq[1]);
      o[2] = bflo(hres[t][1]) + rbf(rbf(sacc[2]) + bq[2]);
      o[3] = bfhi(hres[t][1]) + rbf(rbf(sacc[3]) + bq[3]);
      u32x2 pk = {pk_bf16(o[0], o[1]), pk_bf16(o[2], o[3])};
      *(u32x2*)(out + (size_t)(tb * 64 + t * 16 + l15) * DM + cg * 64 + w * 16 + 4 * g) = pk;
    }
  }
  STAMP(3, 3);
  STAMP_END(3, 4);
}

extern "C" void kernel_launch(void* const* d_in, const int* in_sizes, int n_in,
                              void* d_out, int out_size, void* d_ws, size_t ws_size,
                              hipStream_t stream) {
  const u16* x = (const u16*)d_in[0];
  const u16* lar = (const u16*)d_in[1];
  const u16* aim = (const u16*)d_in[2];
  const u16* cr = (const u16*)d_in[3];
  const u16* ci = (const u16*)d_in[4];
  const u16* ldt = (const u16*)d_in[5];
  const u16* dsk = (const u16*)d_in[6];
  const u16* w1 = (const u16*)d_in[8];
  const u16* b1 = (const u16*)d_in[9];
  const u16* w2 = (const u16*)d_in[10];
  const u16* b2 = (const u16*)d_in[11];
  char* ws = (char*)d_ws;
  k1_prep<<<256, 1024, 0, stream>>>(x, lar, aim, cr, ci, ldt, w1, w2, ws);
  kb_main<<<256, 256, 0, stream>>>(dsk, b1, b2, ws, (u16*)d_out);
}
```

```cpp
#include <hip/hip_runtime.h>
#include <hip/hip_bf16.h>
#include <stdint.h>

typedef unsigned short u16;
typedef __attribute__((ext_vector_type(8))) _Float16 f16x8;
typedef __attribute__((ext_vector_type(2))) _Float16 f16x2;
typedef __attribute__((ext_vector_type(8))) __bf16 bf16x8;
typedef __attribute__((ext_vector_type(2))) __bf16 bf16x2;
typedef __attribute__((ext_vector_type(2))) float f32x2;
typedef __attribute__((ext_vector_type(4))) float f32x4;
typedef __attribute__((ext_vector_type(4))) unsigned u32x4;
typedef __attribute__((ext_vector_type(2))) unsigned u32x2;
typedef __attribute__((ext_vector_type(4))) short s16x4;
#define LDS_AS __attribute__((address_space(3)))

#define SEQ 2048
#define DM 512
#define NM 128
#define FF 1376
#define NFT 86
#define NKS2 43

#define NFTP 88
#define NKSP 44
#define UT_FRONT 512
#define UT_PITCH 4736
#define XT_PITCH 4224
#define WS_SYNC 0
#define WS_UT 8192
#define KR_PITCH 4608
#define WS_KR (WS_UT + DM * UT_PITCH)
#define WS_KSC (WS_KR + DM * KR_PITCH)
#define WS_W1T (WS_KSC + DM * 4)
#define WS_W2T (WS_W1T + NFTP * 16 * 1024)
#define WS_XT (WS_W2T + 32 * NKSP * 1024)
#define WS_HT (WS_XT + DM * XT_PITCH)
#define WS_HACT (WS_HT + DM * XT_PITCH)
#define WS_END (WS_HACT + 128 * NKSP * 1024)

#define STAMP(k, i)
#define STAMP_END(k, i)

__device__ __forceinline__ float bf2f(unsigned v) { return __uint_as_float(v << 16); }
__device__ __forceinline__ float bflo(unsigned v) { return __uint_as_float(v << 16); }
__device__ __forceinline__ float bfhi(unsigned v) { return __uint_as_float(v & 0xffff0000u); }
__device__ __forceinline__ unsigned pk_bf16(float a, float b) {
  f32x2 v = {a, b};
  bf16x2 r = __builtin_convertvector(v, bf16x2);
  return __builtin_bit_cast(unsigned, r);
}
__device__ __forceinline__ unsigned pk_f16(float a, float b) {
  f32x2 v = {a, b};
  f16x2 r = __builtin_convertvector(v, f16x2);
  return __builtin_bit_cast(unsigned, r);
}
__device__ __forceinline__ float rbf(float a) { return bflo(pk_bf16(a, a)); }
__device__ __forceinline__ float h2f_lo(unsigned v) { f16x2 h = __builtin_bit_cast(f16x2, v); return (float)h[0]; }
__device__ __forceinline__ float h2f_hi(unsigned v) { f16x2 h = __builtin_bit_cast(f16x2, v); return (float)h[1]; }
__device__ __forceinline__ unsigned lds_addr(const void* p) { return (unsigned)(size_t)p; }
__device__ __forceinline__ u32x2 tr_read(unsigned ad) {
  s16x4 v = __builtin_amdgcn_ds_read_tr16_b64_v4i16((LDS_AS s16x4*)(size_t)ad);
  return __builtin_bit_cast(u32x2, v);
}
__device__ __forceinline__ void glds16(const void* gsrc, unsigned lds_dst) {
  unsigned keep;
  asm volatile("s_mov_b32 %0, m0\n\ts_mov_b32 m0, %2\n\ts_nop 0\n\tglobal_load_lds_dwordx4 %1, off\n\ts_mov_b32 m0, %0"
               : "=&s"(keep) : "v"(gsrc), "s"(lds_dst) : "memory");
}
__device__ __forceinline__ void glds16_sc1(const void* gsrc, unsigned lds_dst) {
  unsigned keep;
  asm volatile("s_mov_b32 %0, m0\n\ts_mov_b32 m0, %2\n\ts_nop 0\n\tglobal_load_lds_dwordx4 %1, off sc1\n\ts_mov_b32 m0, %0"
               : "=&s"(keep) : "v"(gsrc), "s"(lds_dst) : "memory");
}
__device__ __forceinline__ void store8_dev(void* p, u32x2 v) {
  unsigned long long x = ((unsigned long long)v[1] << 32) | v[0];
  __hip_atomic_store((unsigned long long*)p, x, __ATOMIC_RELAXED, __HIP_MEMORY_SCOPE_AGENT);
}
__device__ __forceinline__ void raw_barrier() {
  asm volatile("s_waitcnt lgkmcnt(0)" ::: "memory");
  __builtin_amdgcn_s_barrier();
  asm volatile("" ::: "memory");
}
template <int CTRL>
__device__ __forceinline__ float dpp_add(float v) {
  int r = __builtin_amdgcn_update_dpp(0, __float_as_int(v), CTRL, 0xf, 0xf, false);
  return v + __int_as_float(r);
}
__device__ __forceinline__ float row16_sum(float v) {
  v = dpp_add<0xB1>(v);
  v = dpp_add<0x4E>(v);
  v = dpp_add<0x141>(v);
  v = dpp_add<0x140>(v);
  return v;
}

struct cpx { float r, i; };
__device__ __forceinline__ cpx cmul(cpx a, cpx b) {
  cpx o; o.r = a.r * b.r - a.i * b.i; o.i = a.r * b.i + a.i * b.r; return o;
}
__device__ __forceinline__ cpx cexp_dir(float lr2, float fr, float e) {
  float mag = __builtin_amdgcn_exp2f(lr2 * e);
  float ph = __builtin_amdgcn_fractf(fr * e);
  cpx o; o.r = mag * __builtin_amdgcn_cosf(ph); o.i = mag * __builtin_amdgcn_sinf(ph); return o;
}

#define KB_CODE_LINES 96
#define K1_AROW 528
#define K1_IMG (32 * K1_AROW)
#define K1_LDS_A 0
#define K1_LDS_B (2 * K1_IMG)
#define K1_KCB 1344
#define K1_LDS_KC (4 * K1_IMG)
#define K1_LDS_RED (K1_LDS_KC + 2 * K1_KCB)
#define K1_LDS_FLAG (K1_LDS_RED + 256)
#define K1_LDS_W (K1_LDS_RED + 272)
#define K1_LDS_SIZE (K1_LDS_W + 4 * 2 * 2560)

extern "C" __global__ __launch_bounds__(1024) void k1_prep(const u16* __restrict__ x, const u16* __restrict__ lar,
                                                const u16* __restrict__ aim, const u16* __restrict__ cre,
                                                const u16* __restrict__ cim, const u16* __restrict__ ldt,
                                                const u16* __restrict__ w1, const u16* __restrict__ w2,
                                                char* __restrict__ ws) {
  __shared__ __attribute__((aligned(16))) char smem[K1_LDS_SIZE];
  const int b = blockIdx.x, tid = threadIdx.x, l = tid & 63;
  const int w = __builtin_amdgcn_readfirstlane(tid >> 6);
  const int role = w >> 2, wr = w & 3;
  const int g = l >> 4, l15 = l & 15;
  float* red = (float*)(smem + K1_LDS_RED);
  volatile LDS_AS unsigned* lnflag = (volatile LDS_AS unsigned*)(size_t)(lds_addr(smem) + K1_LDS_FLAG);
  STAMP(0, 0);

  if (role == 0) {
    const int rb = (b & 7) * 32 + (b >> 3);
    unsigned xv[8];
    float piv[8];
    const unsigned* xr = (const unsigned*)(x + rb * 8 * DM + 128 * wr + 2 * l);
#pragma unroll
    for (int r = 0; r < 8; ++r) xv[r] = xr[r * 256];
#pragma unroll
    for (int r = 0; r < 8; ++r) piv[r] = bf2f(x[(rb * 8 + r) * DM]);
    if (l == 0) lnflag[wr] = 0u;
    raw_barrier();
    const int ch = 128 * wr + 2 * l;
    {
      u32x4 x0, x1;
#pragma unroll
      for (int q = 0; q < 4; ++q) {
        x0[q] = __builtin_amdgcn_perm(xv[2 * q + 1], xv[2 * q], 0x05040100u);
        x1[q] = __builtin_amdgcn_perm(xv[2 * q + 1], xv[2 * q], 0x07060302u);
      }
      char* xp = ws + WS_XT + (size_t)ch * XT_PITCH + rb * 16;
      *(u32x4*)xp = x0;
      *(u32x4*)(xp + XT_PITCH) = x1;
    }
    float s[16];
#pragma unroll
    for (int r = 0; r < 8; ++r) {
      float a = bflo(xv[r]) - piv[r], bb = bfhi(xv[r]) - piv[r];
      s[r] = a + bb;
      s[8 + r] = a * a + bb * bb;
    }
    float t8[8], t4[4];
#pragma unroll
    for (int i = 0; i < 8; ++i) {
      auto sw = __builtin_amdgcn_permlane32_swap(__float_as_uint(s[i]), __float_as_uint(s[8 + i]), false, false);
      t8[i] = __uint_as_float(sw[0]) + __uint_as_float(sw[1]);
    }
#pragma unroll
    for (int i = 0; i < 4; ++i) {
      auto sw = __builtin_amdgcn_permlane16_swap(__float_as_uint(t8[i]), __float_as_uint(t8[4 + i]), false, false);
      t4[i] = row16_sum(__uint_as_float(sw[0]) + __uint_as_float(sw[1]));
    }
    if (l15 == 0) *(f32x4*)(red + wr * 16 + g * 4) = (f32x4){t4[0], t4[1], t4[2], t4[3]};
    asm volatile("s_waitcnt lgkmcnt(0)" ::: "memory");
    if (l == 0) lnflag[wr] = 1u;
    STAMP(0, 1);
#pragma unroll 1
    for (unsigned it = 0; it < (1u << 20); ++it) {
      unsigned f = lnflag[l & 3];
      if (__all(f == 1u)) break;
    }
    asm volatile("" ::: "memory");
    STAMP(0, 2);
    float mb_l, rd_l;
    {
      const int r = l & 7;
      float t1 = red[r] + red[16 + r] + red[32 + r] + red[48 + r];
      float t2 = red[8 + r] + red[24 + r] + red[40 + r] + red[56 + r];
      float pv = piv[0];
#pragma unroll
      for (int k = 1; k < 8; ++k) pv = r == k ? piv[k] : pv;
      float dm = t1 * (1.f / 512.f);
      float var = fmaxf(t2 * (1.f / 512.f) - dm * dm, 0.f);
      mb_l = rbf(pv + dm);
      rd_l = __builtin_amdgcn_rcpf(rbf(__builtin_amdgcn_sqrtf(rbf(rbf(var) + 1e-5f))));
    }
    unsigned ub[8];
#pragma unroll
    for (int r = 0; r < 8; ++r) {
      const float mb = __int_as_float(__builtin_amdgcn_readlane(__float_as_int(mb_l), r));
      const float rd = __int_as_float(__builtin_amdgcn_readlane(__float_as_int(rd_l), r));
      unsigned dd = pk_bf16(bflo(xv[r]) - mb, bfhi(xv[r]) - mb);
      unsigned uu = pk_bf16(bflo(dd) * rd, bfhi(dd) * rd);
      ub[r] = pk_f16(bflo(uu), bfhi(uu));
    }
    u32x4 u0, u1;
#pragma unroll
    for (int q = 0; q < 4; ++q) {
      u0[q] = __builtin_amdgcn_perm(ub[2 * q + 1], ub[2 * q], 0x05040100u);
      u1[q] = __builtin_amdgcn_perm(ub[2 * q + 1], ub[2 * q], 0x07060302u);
    }
    char* up = ws + WS_UT + (size_t)ch * UT_PITCH + UT_FRONT + rb * 16;
    *(u32x4*)up = u0;
    *(u32x4*)(up + UT_PITCH) = u1;
    STAMP(0, 3);
    STAMP_END(0, 6);
    return;
  } else if (role == 3) {
    const int gw = b * 4 + wr;
    char* wl = smem + K1_LDS_W + wr * 5120;
    unsigned cpf0 = 0;
    if (wr == 1) {
      unsigned long long ca;
      unsigned rlo, rhi;
      asm volatile("s_getpc_b64 %0\n\ts_mov_b32 %1, k1_prep@rel32@lo+4\n\ts_mov_b32 %2, k1_prep@rel32@hi+12" : "=&s"(ca), "=&s"(rlo), "=&s"(rhi));
      ca += ((unsigned long long)rhi << 32) | rlo;
      cpf0 = *(const unsigned*)((const char*)ca + l * 128);
    }
    u32x4 v[2][2];
#pragma unroll
    for (int it = 0; it < 2; ++it) {
      const int bi = gw + it * 1024;
      if (bi < 688) {
        const int ks = bi / 43, fb = bi - ks * 43;
        const u16* src = w1 + (size_t)(32 * ks + (l >> 2)) * FF + 32 * fb + 8 * (l & 3);
        v[it][0] = *(const u32x4*)src;
        v[it][1] = *(const u32x4*)(src + 16 * FF);
      } else if (bi < 1376) {
        const int b2 = bi - 688, ks = b2 >> 4, nbk = b2 & 15;
        const u16* src = w2 + (size_t)(32 * ks + (l >> 2)) * DM + 32 * nbk + 8 * (l & 3);
        v[it][0] = *(const u32x4*)src;
        v[it][1] = *(const u32x4*)(src + 16 * DM);
      }
    }
    raw_barrier();
    if (wr == 0 && l < 40) {
#pragma unroll
      for (int cc = 0; cc < 2; ++cc)
        *(u32x4*)(ws + WS_UT + (size_t)(2 * b + cc) * UT_PITCH + (l < 32 ? l * 16 : 4608 + (l - 32) * 16)) = (u32x4){0, 0, 0, 0};
    }
    unsigned cpf = 0;
    if (wr == 3) {
      unsigned long long ca;
      unsigned rlo, rhi;
      asm volatile("s_getpc_b64 %0\n\ts_mov_b32 %1, kb_main@rel32@lo+4\n\ts_mov_b32 %2, kb_main@rel32@hi+12" : "=&s"(ca), "=&s"(rlo), "=&s"(rhi));
      ca += ((unsigned long long)rhi << 32) | rlo;
      const int li = ((b >> 3) & 1) * 64 + l;
      cpf = *(const unsigned*)((const char*)ca + (li < KB_CODE_LINES ? li : 0) * 128);
    }
    if (wr == 2) {
      if (b < 32) *(u32x4*)(ws + WS_W1T + (size_t)(86 * 16 + b) * 1024 + l * 16) = (u32x4){0, 0, 0, 0};
      else if (b < 64) *(u32x4*)(ws + WS_W2T + (size_t)((b - 32) * NKSP + 43) * 1024 + l * 16) = (u32x4){0, 0, 0, 0};
      else if (b < 72) *(u32x4*)(ws + WS_SYNC + (b - 64) * 1024 + l * 16) = (u32x4){0, 0, 0, 0};
    }
#pragma unroll
    for (int it = 0; it < 2; ++it) {
      const int bi = gw + it * 1024;
      if (bi < 1376) {
        char* t = wl + it * 2560;
        *(u32x4*)(t + (l >> 2) * 80 + (l & 3) * 16) = v[it][0];
        *(u32x4*)(t + (16 + (l >> 2)) * 80 + (l & 3) * 16) = v[it][1];
        const bool isw1 = bi < 688;
        const int q = l15 >> 2, p = l15 & 3;
        const unsigned a0 = lds_addr(t) + (isw1 ? (8 * g + q) : (4 * g + q)) * 80 + p * 8;
        const unsigned dr = isw1 ? 4 * 80 : 16 * 80;
#pragma unroll
        for (int ct = 0; ct < 2; ++ct) {
          u32x2 lo = tr_read(a0 + ct * 32), hi = tr_read(a0 + ct * 32 + dr);
          u32x4 o = {lo[0], lo[1], hi[0], hi[1]};
          if (isw1) {
            const int ks = bi / 43, fb = bi - ks * 43, ft = 2 * fb + ct;
            __builtin_nontemporal_store(o, (u32x4*)(ws + WS_W1T + (size_t)(ft * 16 + ks) * 1024 + l * 16));
          } else {
            const int b2 = bi - 688, ks = b2 >> 4, nt = 2 * (b2 & 15) + ct;
            __builtin_nontemporal_store(o, (u32x4*)(ws + WS_W2T + (size_t)(nt * NKSP + ks) * 1024 + l * 16));
          }
        }
      }
    }
    asm volatile("" :: "v"(cpf), "v"(cpf0));
    STAMP(0, 3);
    STAMP_END(0, 6);
    return;
  } else {
    const int c = role - 1, part = wr, d = 2 * b + c;
    const unsigned v_lar = *(const unsigned*)(lar + d * NM + 2 * l), v_aim = *(const unsigned*)(aim + d * NM + 2 * l);
    const unsigned v_cr = *(const unsigned*)(cre + d * NM + 2 * l), v_ci = *(const unsigned*)(cim + d * NM + 2 * l);
    const unsigned v_ldt = ldt[d];
    asm volatile("" ::: "memory");
    raw_barrier();
    const float dt = rbf(__expf(bf2f(v_ldt)));
    float lr2[2], fr[2];
    lr2[0] = (-rbf(__expf(bflo(v_lar))) * dt) * 1.4426950408889634f;
    lr2[1] = (-rbf(__expf(bfhi(v_lar))) * dt) * 1.4426950408889634f;
    fr[0] = (bflo(v_aim) * dt) * 0.15915494309189535f;
    fr[1] = (bfhi(v_aim) * dt) * 0.15915494309189535f;
    if (part == 3 && l < 2) *(u32x4*)(smem + K1_LDS_KC + c * K1_KCB + l * 16) = (u32x4){0, 0, 0, 0};
    if (part < 2) {
      float cm = fmaxf(fmaxf(fabsf(bflo(v_cr)), fabsf(bfhi(v_cr))), fmaxf(fabsf(bflo(v_ci)), fabsf(bfhi(v_ci))));
#pragma unroll
      for (int o = 32; o > 0; o >>= 1) cm = fmaxf(cm, __shfl_xor(cm, o));
      const float cinv = cm > 0.f ? 1.f / cm : 0.f;
      char* Aimg = smem + K1_LDS_A + c * K1_IMG + (16 * part) * K1_AROW + l * 8;
      cpx P[2][2], z8[2];
      const float e0 = 128.f * part;
#pragma unroll
      for (int m = 0; m < 2; ++m) {
        cpx C; C.r = (m ? bfhi(v_cr) : bflo(v_cr)) * cinv; C.i = (m ? bfhi(v_ci) : bflo(v_ci)) * cinv;
        z8[m] = cexp_dir(lr2[m], fr[m], 8.f);
        P[m][0] = cmul(C, cexp_dir(lr2[m], fr[m], e0));
        P[m][1] = cmul(C, cexp_dir(lr2[m], fr[m], e0 + 64.f));
      }
#pragma unroll
      for (int a = 0; a < 8; ++a) {
#pragma unroll
        for (int k = 0; k < 2; ++k) {
          u32x2 o = {pk_f16(P[0][k].r, P[0][k].i), pk_f16(P[1][k].r, P[1][k].i)};
          *(u32x2*)(Aimg + (8 * k + a) * K1_AROW) = o;
          if (a < 7) { P[0][k] = cmul(P[0][k], z8[0]); P[1][k] = cmul(P[1][k], z8[1]); }
        }
      }
      if (part == 0 && l == 0) ((float*)(ws + WS_KSC))[d] = 2.f * dt * cm * 128.f;
    } else {
      char* Bimg = smem + K1_LDS_B + c * K1_IMG + l * 8;
#define PUTB(col, v0, v1) { cpx _a = (v0), _b = (v1); u32x2 _o = {pk_f16(_a.r, -_a.i), pk_f16(_b.r, -_b.i)}; *(u32x2*)(Bimg + (col) * K1_AROW) = _o; }
      if (part == 2) {
        cpx z[2][8], e256[2];
#pragma unroll
        for (int m = 0; m < 2; ++m) {
          z[m][0].r = 1.f; z[m][0].i = 0.f;
          z[m][1] = cexp_dir(lr2[m], fr[m], 1.f);
          z[m][2] = cmul(z[m][1], z[m][1]); z[m][3] = cmul(z[m][2], z[m][1]); z[m][4] = cmul(z[m][2], z[m][2]);
          z[m][5] = cmul(z[m][4], z[m][1]); z[m][6] = cmul(z[m][4], z[m][2]); z[m][7] = cmul(z[m][4], z[m][3]);
          e256[m] = cexp_dir(lr2[m], fr[m], 256.f);
        }
#pragma unroll
        for (int k = 0; k < 8; ++k) PUTB(k, z[0][k], z[1][k]);
        PUTB(8, e256[0], e256[1]);
#pragma unroll
        for (int k = 1; k < 4; ++k) PUTB(8 + k, cmul(e256[0], z[0][2 * k]), cmul(e256[1], z[1][2 * k]));
      } else {
        cpx z4[2], e256[2], e512[2], e768[2], e1024[2];
#pragma unroll
        for (int m = 0; m < 2; ++m) {
          z4[m] = cexp_dir(lr2[m], fr[m], 4.f);
          e256[m] = cexp_dir(lr2[m], fr[m], 256.f);
          e1024[m] = cexp_dir(lr2[m], fr[m], 1024.f);
          e512[m] = cmul(e256[m], e256[m]); e768[m] = cmul(e512[m], e256[m]);
        }
        PUTB(12, e512[0], e512[1]); PUTB(13, cmul(e512[0], z4[0]), cmul(e512[1], z4[1]));
        PUTB(14, e768[0], e768[1]); PUTB(15, cmul(e768[0], z4[0]), cmul(e768[1], z4[1]));
        PUTB(16, e1024[0], e1024[1]);
        PUTB(17, cmul(e1024[0], e256[0]), cmul(e1024[1], e256[1]));
        PUTB(18, cmul(e1024[0], e512[0]), cmul(e1024[1], e512[1]));
        PUTB(19, cmul(e1024[0], e768[0]), cmul(e1024[1], e768[1]));
        PUTB(20, cmul(e1024[0], e1024[0]), cmul(e1024[1], e1024[1]));
      }
#undef PUTB
    }
    STAMP(0, 1);
    raw_barrier();
    STAMP(0, 2);
    {
      const int rt = wr >> 1, ct = wr & 1;
      const char* Ab = smem + K1_LDS_A + c * K1_IMG + (16 * rt + l15) * K1_AROW + g * 16;
      const int bc = 16 * ct + l15;
      const char* Bb = smem + K1_LDS_B + c * K1_IMG + (bc < 21 ? bc : 20) * K1_AROW + g * 16;
      f16x8 af[8], bfr[8];
#pragma unroll
      for (int q = 0; q < 8; ++q) { af[q] = *(const f16x8*)(Ab + q * 64); bfr[q] = *(const f16x8*)(Bb + q * 64); }
      f32x4 acc = {0, 0, 0, 0};
#pragma unroll
      for (int q = 0; q < 8; ++q) acc = __builtin_amdgcn_mfma_f32_16x16x32_f16(af[q], bfr[q], acc, 0, 0, 0);
      _Float16* kc = (_Float16*)(smem + K1_LDS_KC + c * K1_KCB + 32);
      const int a0 = 16 * rt + 4 * g;
      int base, stride;
      if (ct == 0) {
        if (l15 < 8) { base = l15; stride = 8; }
        else if (l15 < 12) { base = 256 + (l15 - 8); stride = 4; }
        else { base = 384 + 64 * ((l15 - 12) >> 1) + (l15 & 1); stride = 2; }
      } else {
        base = 512 + 32 * l15; stride = 1;
      }
#pragma unroll
      for (int r = 0; r < 4; ++r) {
        const bool ok = ct == 0 || l15 < 4 || (l15 == 4 && a0 + r == 0);
        if (ok) kc[base + stride * (a0 + r)] = (_Float16)(acc[r] * (1.f / 128.f));
      }
    }
    STAMP(0, 3);
  }
  raw_barrier();
  STAMP(0, 4);
  if (tid - 256 < 168) *(u32x4*)(ws + WS_KR + (size_t)b * 2 * K1_KCB + (tid - 256) * 16) = *(const u32x4*)(smem + K1_LDS_KC + (tid - 256) * 16);
  STAMP(0, 5);
  STAMP_END(0, 6);
}

#define K2_ROWB 272
#define K2_COPY (18 * K2_ROWB)
#define K2_UCH (4 * K2_COPY)
#define K2_LDS_U 0
#define K2_LDS_K (39 * 1024)
#define K2_LDS_X (K2_LDS_K + 2 * 4608)
#define KG_KCB 1344
#define KG_LDS_KC (K2_LDS_X + 8192)
#define K2_LDS_Y (KG_LDS_KC + 3072)
#define K3_LDS_H 0
#define K3_LDS_E 65536
#define K3_LDS_ST (K3_LDS_E + 8 * 9216)
#define K4_LDS_R 0
#define KB_LDS_SIZE (K3_LDS_ST + 1024)
#define SPIN_MAX (1u << 22)

__device__ __forceinline__ f16x8 ld_g(const char* p) {
  u32x2 lo = *(const u32x2*)p, hi = *(const u32x2*)(p + 8);
  u32x4 t = {lo[0], lo[1], hi[0], hi[1]};
  return __builtin_bit_cast(f16x8, t);
}
__device__ __forceinline__ bf16x8 ld_bfrag(unsigned ad) {
  u32x2 l2 = tr_read(ad), h2 = tr_read(ad + 512);
  u32x4 tt = {l2[0], l2[1], h2[0], h2[1]};
  return __builtin_bit_cast(bf16x8, tt);
}

extern "C" __global__ __launch_bounds__(512, 2) void kb_main(const u16* __restrict__ dsk,
                                               const u16* __restrict__ b1, const u16* __restrict__ b2,
                                               char* __restrict__ ws, u16* __restrict__ out) {
  __shared__ __attribute__((aligned(16))) char smem[KB_LDS_SIZE];
  const int b = blockIdx.x, tid = threadIdx.x, l = tid & 63;
  const int w = __builtin_amdgcn_readfirstlane(tid >> 6);
  const int g = l >> 4, l15 = l & 15;
  const int cp = b;
  const int tb = (b & 7) * 4 + ((b >> 3) & 3);
  const int cg = b >> 5;
  const int wl = w & 3, wh = w >> 2;
  const unsigned sb = lds_addr(smem);
  unsigned* sync = (unsigned*)(ws + WS_SYNC);
  STAMP(1, 0);
  if (w < 3) {
    const int o = w * 1024 + l * 16;
    glds16(ws + WS_KR + (size_t)cp * 2 * KG_KCB + (o < 2688 ? o : 0), sb + KG_LDS_KC + w * 1024);
  }
  {
    const char* ub = ws + WS_UT + (size_t)(2 * cp + wh) * UT_PITCH + wl * 2;
    const unsigned ud = sb + K2_LDS_U + w * K2_COPY;
#pragma unroll
    for (int it = 0; it < 5; ++it) {
      const int i = it * 64 + l;
      const int row = (i * 241) >> 12, q = i - row * 17;
      if (it < 4 || l < 50) glds16(ub + (128 * row + 8 * q) * 2, ud + it * 1024);
    }
    glds16(ws + WS_XT + (size_t)(2 * cp + wh) * XT_PITCH + wl * 1024 + l * 16, sb + K2_LDS_X + w * 1024);
  }
  const int myft = cg * 11 + 3 * wl;
  bf16x8 wf[3][8];
  const __amdgpu_buffer_rsrc_t rw1 = __builtin_amdgcn_make_buffer_rsrc(ws + WS_W1T, 0, NFTP * 16 * 1024, 0x00020000);
  const unsigned w1o = (myft * 16 + wh * 8) * 1024;
  const unsigned w1o2 = w1o + (wl < 3 ? 2 : 1) * 16 * 1024;
#define LDW1(a, k) __builtin_bit_cast(bf16x8, __builtin_amdgcn_raw_buffer_load_b128(rw1, l * 16, ((a) == 2 ? w1o2 : w1o + (a) * 16 * 1024) + (k) * 1024, 0))
  asm volatile("s_waitcnt vmcnt(6)" ::: "memory");
  raw_barrier();
  STAMP(1, 8);
  const float ksc = ((const float*)(ws + WS_KSC))[2 * cp + wh];
  const unsigned dkv = *(const unsigned*)(dsk + 2 * cp);
#pragma unroll
  for (int it = 0; it < 2; ++it) {
    const int ei = tid + it * 512;
    if (ei < 550) {
      const int cc = ei >= 275, ck = ei - cc * 275;
      int c2 = 259 - ck;
      const bool top = c2 == 256;
      if (c2 > 256 || c2 < 0) c2 = -1;
      int ih; unsigned tbl;
      if (c2 <= 32) { ih = 8 * c2; tbl = 0x76543210u; }
      else if (c2 <= 64) { ih = 256 + 4 * (c2 - 32); tbl = 0x43222100u; }
      else if (c2 <= 128) { ih = 384 + 2 * (c2 - 64); tbl = 0x22111000u; }
      else { ih = 512 + (c2 - 128); tbl = 0x11100000u | ((unsigned)(c2 & 1) << 16); }
      const u16* kc = (const u16*)(smem + KG_LDS_KC + cc * KG_KCB + 32) + ih;
      unsigned v[8];
#pragma unroll
      for (int j = 0; j < 8; ++j) v[j] = kc[-(int)((tbl >> (4 * j)) & 15u)];
      if (top) v[0] = 0u;
      u32x4 o = {v[0] | (v[1] << 16), v[2] | (v[3] << 16), v[4] | (v[5] << 16), v[6] | (v[7] << 16)};
      *(u32x4*)(smem + K2_LDS_K + cc * 4608 + ck * 16) = o;
    }
  }
  STAMP(1, 7);
  asm volatile("s_waitcnt vmcnt(0)" ::: "memory");
  raw_barrier();
  STAMP(1, 1);
  {
    f32x4 acc[8];
#pragma unroll
    for (int i = 0; i < 8; ++i) acc[i] = (f32x4){0, 0, 0, 0};
    const int phi = l15 & 7, nu = l15 >> 3;
    const char* Fb = smem + K2_LDS_K + wh * 4608 + (259 - l15 + g) * 16 + 192 - 64 * wl;
    const char* Gb = smem + K2_LDS_U + wh * K2_UCH + (phi & 3) * K2_COPY + (nu + 2) * K2_ROWB + ((phi & 4) + 8 * g) * 2 + 192 - 64 * wl;
#pragma unroll
    for (int rr = 0; rr < 2; ++rr) {
      f16x8 F[8], G[8];
#pragma unroll
      for (int q = 0; q < 8; ++q) { G[q] = ld_g(Gb - rr * K2_ROWB + q * 2 * K2_ROWB); F[q] = *(const f16x8*)(Fb - rr * 256 - q * 512); }
      __builtin_amdgcn_sched_barrier(0);
#pragma unroll
      for (int k = 0; k < 8; ++k) wf[rr][k] = LDW1(rr, k);
#pragma unroll
      for (int e = 0; e < 8; ++e)
#pragma unroll
        for (int q = 0; q + e < 8; ++q)
          acc[q + e] = __builtin_amdgcn_mfma_f32_16x16x32_f16(G[e], F[q], acc[q + e], 0, 0, 0);
#pragma unroll
      for (int k = 0; k < 8; ++k) {
        __builtin_amdgcn_sched_group_barrier(0x008, 4, 0);
        __builtin_amdgcn_sched_group_barrier(0x020, 1, 0);
      }
      __builtin_amdgcn_sched_barrier(0);
      if (rr == 0 && wl == 0) {
        f16x8 Gm = ld_g(Gb - 2 * K2_ROWB), F8 = *(const f16x8*)(Fb - 8 * 512);
#pragma unroll
        for (int i = 0; i < 7; ++i) acc[i] = __builtin_amdgcn_mfma_f32_16x16x32_f16(Gm, F[i + 1], acc[i], 0, 0, 0);
        acc[7] = __builtin_amdgcn_mfma_f32_16x16x32_f16(Gm, F8, acc[7], 0, 0, 0);
      }
    }
    STAMP(1, 2);
#pragma unroll
    for (int i = 0; i < 8; ++i) *(f32x4*)(smem + K2_LDS_Y + (w * 8 + i) * 1024 + l * 16) = acc[i];
    raw_barrier();
    const float dk = wh ? bfhi(dkv) : bflo(dkv);
    const int tl = 128 * (g >> 1) + 8 * l15 + 4 * (g & 1);
#pragma unroll
    for (int i = 0; i < 2; ++i) {
      const int ti = 2 * wl + i;
      f32x4 o = *(const f32x4*)(smem + K2_LDS_Y + ((wh * 4 + 0) * 8 + ti) * 1024 + l * 16);
#pragma unroll
      for (int k = 1; k < 4; ++k) o += *(const f32x4*)(smem + K2_LDS_Y + ((wh * 4 + k) * 8 + ti) * 1024 + l * 16);
      const int t0 = 256 * ti + tl;
      u32x2 uu = *(const u32x2*)(smem + K2_LDS_U + wh * K2_UCH + ((t0 >> 7) + 2) * K2_ROWB + (t0 & 127) * 2);
      u32x2 xx = *(const u32x2*)(smem + K2_LDS_X + wh * 4096 + t0 * 2);
      u32x2 res;
#pragma unroll
      for (int j = 0; j < 2; ++j) {
        unsigned yy = pk_bf16(o[2 * j] * ksc, o[2 * j + 1] * ksc);
        unsigned ud = pk_bf16(h2f_lo(uu[j]) * dk, h2f_hi(uu[j]) * dk);
        unsigned z = pk_bf16(bflo(yy) + bflo(ud), bfhi(yy) + bfhi(ud));
        res[j] = pk_bf16(bflo(xx[j]) + bflo(z), bfhi(xx[j]) + bfhi(z));
      }
      store8_dev(ws + WS_HT + (size_t)(2 * cp + wh) * XT_PITCH + t0 * 2, res);
    }
  }
  STAMP(1, 3);
  asm volatile("s_waitcnt vmcnt(0)" ::: "memory");
  raw_barrier();
  if (tid == 0) __hip_atomic_fetch_add(sync + (b & 7) * 32, 1u, __ATOMIC_RELAXED, __HIP_MEMORY_SCOPE_AGENT);
  STAMP(1, 4);
  const int ks0 = w * 5 + (w < 3 ? w : 3);
  bf16x8 w2f[6][4];
  const __amdgpu_buffer_rsrc_t rw2 = __builtin_amdgcn_make_buffer_rsrc(ws + WS_W2T, 0, 32 * NKSP * 1024, 0x00020000);
  const unsigned w2o = ((cg * 4) * NKSP + ks0) * 1024;
#define LDW2(k, a) __builtin_bit_cast(bf16x8, __builtin_amdgcn_raw_buffer_load_b128(rw2, l * 16, w2o + ((a) * NKSP + (k)) * 1024, 0))
#pragma unroll
  for (int k = 0; k < 8; ++k) wf[2][k] = LDW1(2, k);
  const u32x2 b2v = *(const u32x2*)(b2 + cg * 64 + (w >> 1) * 16 + 4 * g);
  u32x2 b1v[3];
#pragma unroll
  for (int a = 0; a < 3; ++a) {
    const int f = 16 * (myft + a) + 4 * g;
    b1v[a] = *(const u32x2*)(b1 + (f < FF ? f : 0));
  }
  __builtin_amdgcn_sched_barrier(0);
  const u32x4 onesu = {0x3f803f80u, 0x3f803f80u, 0x3f803f80u, 0x3f803f80u};
  const bf16x8 onesf = __builtin_bit_cast(bf16x8, onesu);
  {
    f32x4 cq[2];
#pragma unroll
    for (int a = 0; a < 2; ++a) cq[a] = (f32x4){0, 0, 0, 0};
#pragma unroll
    for (int k = 0; k < 8; ++k)
#pragma unroll
      for (int a = 0; a < 2; ++a) cq[a] = __builtin_amdgcn_mfma_f32_16x16x32_bf16(wf[a][k], onesf, cq[a], 0, 0, 0);
#pragma unroll
    for (int a = 0; a < 2; ++a) *(f32x4*)(smem + K3_LDS_E + (w * 9 + 6 + a) * 1024 + l * 16) = cq[a];
  }
  STAMP(1, 5);
  if (w == 0) {
#pragma unroll 1
    for (unsigned it = 0; it < SPIN_MAX; ++it) {
      unsigned v = __hip_atomic_load(sync + (l & 7) * 32, __ATOMIC_RELAXED, __HIP_MEMORY_SCOPE_AGENT);
      if (__all(v >= 32u)) break;
      __builtin_amdgcn_s_sleep(1);
    }
  }
  raw_barrier();
  STAMP(2, 0);
  asm volatile("" : "+v"(wf[2][0]), "+v"(wf[2][1]), "+v"(wf[2][2]), "+v"(wf[2][3]), "+v"(wf[2][4]), "+v"(wf[2][5]), "+v"(wf[2][6]), "+v"(wf[2][7]) : "v"(b2v), "v"(b1v[0]), "v"(b1v[1]), "v"(b1v[2]) : "memory");
#pragma unroll
  for (int it = 0; it < 8; ++it) {
    const int j = it * 8 + w;
    const int row = j * 8 + (l >> 3), pc = l & 7;
    const int swz = ((row >> 1) & 1) | (((row >> 3) & 1) << 1);
    const int lc = (((pc >> 1) ^ swz) << 1) | (pc & 1);
    glds16_sc1(ws + WS_HT + (size_t)row * XT_PITCH + tb * 128 + lc * 16, sb + K3_LDS_H + j * 1024);
  }
  {
    f32x4 cq = {0, 0, 0, 0};
#pragma unroll
    for (int k = 0; k < 8; ++k) cq = __builtin_amdgcn_mfma_f32_16x16x32_bf16(wf[2][k], onesf, cq, 0, 0, 0);
    *(f32x4*)(smem + K3_LDS_E + (w * 9 + 8) * 1024 + l * 16) = cq;
  }
  asm volatile("s_waitcnt vmcnt(0)" ::: "memory");
  raw_barrier();
  STAMP(2, 1);
  const int q4 = l15 >> 2, p4 = l15 & 3;
  const __amdgpu_buffer_rsrc_t rs = __builtin_amdgcn_make_buffer_rsrc(ws + WS_HACT, 0, 64 * NFTP * 1024, 0x00020000);
  unsigned* pcA = sync + 256 + tb * 32;
  u32x2 hres[2];
  {
    const int swz = ((q4 >> 1) & 1) | ((g & 1) << 1);
    const unsigned lbase = sb + K3_LDS_H + wh * 8 * 4096 + (8 * g + q4) * 128 + p4 * 8;
    unsigned ad[4];
#pragma unroll
    for (int t = 0; t < 4; ++t) ad[t] = lbase + (((t ^ (2 * wh)) ^ swz) << 5);
    const unsigned sa = lbase + ((wl ^ swz) << 5);
    const unsigned vo2 = wl < 3 ? (unsigned)(l * 16) : 0x7f000000u;
    f32x4 acc[3][2];
    bf16x8 bf[2][2];
    auto finish = [&](int p) {
      f32x4 cs[3], bb[3];
      f32x2 st[2];
#pragma unroll
      for (int a = 0; a < 3; ++a) {
        bb[a] = (f32x4){bflo(b1v[a][0]), bfhi(b1v[a][0]), bflo(b1v[a][1]), bfhi(b1v[a][1])};
        cs[a] = *(const f32x4*)(smem + K3_LDS_E + (w * 9 + 6 + a) * 1024 + l * 16) + *(const f32x4*)(smem + K3_LDS_E + ((w ^ 4) * 9 + 6 + a) * 1024 + l * 16);
      }
#pragma unroll
      for (int t = 0; t < 2; ++t) {
        const int tr = (2 * p + t) * 16 + l15;
        f32x2 p0 = *(const f32x2*)(smem + K3_LDS_ST + tr * 8), p1 = *(const f32x2*)(smem + K3_LDS_ST + (64 + tr) * 8);
        float mean = (p0[0] + p1[0]) * (1.f / 512.f);
        float var = fmaxf((p0[1] + p1[1]) * (1.f / 512.f) - mean * mean, 0.f);
        float m1 = rbf(mean);
        float r1 = __builtin_amdgcn_rcpf(rbf(__builtin_amdgcn_sqrtf(rbf(rbf(var) + 1e-5f))));
        float mean2 = (mean - m1) * r1, var2 = var * r1 * r1;
        float m2 = rbf(mean2);
        float r2 = __builtin_amdgcn_rcpf(rbf(__builtin_amdgcn_sqrtf(rbf(rbf(var2) + 1e-5f))));
        st[t] = (f32x2){r1 * r2, -(m1 * r1 + m2) * r2};
      }
#pragma unroll
      for (int a = 0; a < 3; ++a)
#pragma unroll
        for (int t = 0; t < 2; ++t) acc[a][t] += *(const f32x4*)(smem + K3_LDS_E + ((w ^ 4) * 9 + a * 2 + t) * 1024 + l * 16);
      const unsigned so = ((tb * 2 + p) * NFTP + myft) * 1024;
      const f32x2 c1 = {-0.10294324f, -0.10294324f}, c0 = {-2.30220819f, -2.30220819f}, one = {1.f, 1.f};
      f32x2 v[12], q[12];
#pragma unroll
      for (int i = 0; i < 12; ++i) {
        const int a = i >> 2, t = (i >> 1) & 1, r = 2 * (i & 1);
        const f32x2 a2 = {st[t][0], st[t][0]}, b2 = {st[t][1], st[t][1]};
        v[i] = __builtin_elementwise_fma(a2, (f32x2){acc[a][t][r], acc[a][t][r + 1]},
                                         __builtin_elementwise_fma(b2, (f32x2){cs[a][r], cs[a][r + 1]}, (f32x2){bb[a][r], bb[a][r + 1]}));
      }
      __builtin_amdgcn_sched_barrier(0);
#pragma unroll
      for (int i = 0; i < 12; ++i) q[i] = v[i] * v[i];
      __builtin_amdgcn_sched_barrier(0);
#pragma unroll
      for (int i = 0; i < 12; ++i) q[i] = __builtin_elementwise_fma(q[i], c1, c0);
      __builtin_amdgcn_sched_barrier(0);
#pragma unroll
      for (int i = 0; i < 12; ++i) q[i] = q[i] * v[i];
      __builtin_amdgcn_sched_barrier(0);
#pragma unroll
      for (int i = 0; i < 12; ++i) q[i] = (f32x2){__builtin_amdgcn_exp2f(q[i][0]), __builtin_amdgcn_exp2f(q[i][1])};
      __builtin_amdgcn_sched_barrier(0);
#pragma unroll
      for (int i = 0; i < 12; ++i) q[i] = q[i] + one;
      __builtin_amdgcn_sched_barrier(0);
#pragma unroll
      for (int i = 0; i < 12; ++i) q[i] = (f32x2){__builtin_amdgcn_rcpf(q[i][0]), __builtin_amdgcn_rcpf(q[i][1])};
      __builtin_amdgcn_sched_barrier(0);
#pragma unroll
      for (int i = 0; i < 12; ++i) v[i] = v[i] * q[i];
      __builtin_amdgcn_sched_barrier(0);
#pragma unroll
      for (int a = 0; a < 3; ++a) {
        u32x4 o = {pk_bf16(v[4 * a][0], v[4 * a][1]), pk_bf16(v[4 * a + 1][0], v[4 * a + 1][1]),
                   pk_bf16(v[4 * a + 2][0], v[4 * a + 2][1]), pk_bf16(v[4 * a + 3][0], v[4 * a + 3][1])};
        __builtin_amdgcn_raw_buffer_store_b128(o, rs, (a < 2 ? (unsigned)(l * 16) : vo2) + so + a * 1024, 0, 16);
      }
      __builtin_amdgcn_sched_barrier(0);
    };
    {
      f32x4 sS = {0, 0, 0, 0}, sQ = {0, 0, 0, 0};
      f32x4 acs[3][2];
      bf16x8 bg[2][2], sf[2];
#pragma unroll
      for (int a = 0; a < 3; ++a)
#pragma unroll
        for (int t = 0; t < 2; ++t) { acc[a][t] = (f32x4){0, 0, 0, 0}; acs[a][t] = (f32x4){0, 0, 0, 0}; }
      bf[0][0] = ld_bfrag(ad[0]); bf[0][1] = ld_bfrag(ad[1]); bg[0][0] = ld_bfrag(ad[2]); bg[0][1] = ld_bfrag(ad[3]); sf[0] = ld_bfrag(sa);
      __builtin_amdgcn_sched_barrier(0);
#pragma unroll
      for (int ks = 0; ks < 8; ++ks) {
        const int cur = ks & 1, nx = cur ^ 1;
        if (ks < 7) {
          bf[nx][0] = ld_bfrag(ad[0] + (ks + 1) * 4096); bf[nx][1] = ld_bfrag(ad[1] + (ks + 1) * 4096);
          bg[nx][0] = ld_bfrag(ad[2] + (ks + 1) * 4096); bg[nx][1] = ld_bfrag(ad[3] + (ks + 1) * 4096);
          sf[nx] = ld_bfrag(sa + (ks + 1) * 4096);
        }
        w2f[ks >> 1][(ks & 1) * 2] = LDW2(ks >> 1, (ks & 1) * 2);
        w2f[ks >> 1][(ks & 1) * 2 + 1] = LDW2(ks >> 1, (ks & 1) * 2 + 1);
#pragma unroll
        for (int t = 0; t < 2; ++t)
#pragma unroll
          for (int a = 0; a < 3; ++a) acs[a][t] = __builtin_amdgcn_mfma_f32_16x16x32_bf16(wf[a][ks], bg[cur][t], acs[a][t], 0, 0, 0);
#pragma unroll
        for (int t = 0; t < 2; ++t)
#pragma unroll
          for (int a = 0; a < 3; ++a) acc[a][t] = __builtin_amdgcn_mfma_f32_16x16x32_bf16(wf[a][ks], bf[cur][t], acc[a][t], 0, 0, 0);
        sS = __builtin_amdgcn_mfma_f32_16x16x32_bf16(onesf, sf[cur], sS, 0, 0, 0);
        sQ = __builtin_amdgcn_mfma_f32_16x16x32_bf16(sf[cur], sf[cur], sQ, 0, 0, 0);
#pragma unroll
        for (int k = 0; k < 10; ++k) {
          __builtin_amdgcn_sched_group_barrier(0x008, 1, 0);
          if (ks < 7) __builtin_amdgcn_sched_group_barrier(0x100, 1, 0);
          if (k == 0 || k == 6) __builtin_amdgcn_sched_group_barrier(0x020, 1, 0);
        }
        __builtin_amdgcn_sched_barrier(0);
      }
      STAMP(2, 2);
#pragma unroll
      for (int a = 0; a < 3; ++a)
#pragma unroll
        for (int t = 0; t < 2; ++t) *(f32x4*)(smem + K3_LDS_E + (w * 9 + a * 2 + t) * 1024 + l * 16) = acs[a][t];
      float dq = (l15 & 2) ? ((l15 & 1) ? sQ[3] : sQ[2]) : ((l15 & 1) ? sQ[1] : sQ[0]);
      float s2 = __shfl(dq, l15 + 16 * (l15 >> 2));
      if (l < 16) *(f32x2*)(smem + K3_LDS_ST + (wh * 64 + wl * 16 + l) * 8) = (f32x2){sS[0], s2};
    }
#pragma unroll
    for (int a = 0; a < 4; ++a) w2f[4][a] = LDW2(4, a);
    if (w < 3) {
#pragma unroll
      for (int a = 0; a < 4; ++a) w2f[5][a] = LDW2(5, a);
    }
    {
      const int row = cg * 64 + (w >> 1) * 16 + 4 * g + q4;
      const int swz2 = ((row >> 1) & 1) | (((row >> 3) & 1) << 1);
      const unsigned hb = sb + K3_LDS_H + row * 128 + p4 * 8;
#pragma unroll
      for (int i = 0; i < 2; ++i) hres[i] = tr_read(hb + (((2 * i + (w & 1)) ^ swz2) << 5));
    }
    raw_barrier();
    STAMP(2, 3);
    finish(wh);
  }
  STAMP(2, 4);
  asm volatile("s_waitcnt vmcnt(0)" ::: "memory");
  raw_barrier();
  STAMP(2, 5);
  if (w == 0) {
    if (l == 0) __hip_atomic_fetch_add(pcA, 1u, __ATOMIC_RELAXED, __HIP_MEMORY_SCOPE_AGENT);
#pragma unroll 1
    for (unsigned it = 0; it < SPIN_MAX; ++it) {
      unsigned v = __hip_atomic_load(pcA, __ATOMIC_RELAXED, __HIP_MEMORY_SCOPE_AGENT);
      if (v >= 8u) break;
      __builtin_amdgcn_s_sleep(1);
    }
  }
  raw_barrier();
  STAMP(3, 0);
  {
    const unsigned hvo = ((tb * 2) * NFTP + ks0 * 2) * 1024;
    u32x4 hv[2][6][2];
#pragma unroll
    for (int p = 0; p < 2; ++p) {
#pragma unroll
      for (int k = 0; k < 5; ++k)
#pragma unroll
        for (int f = 0; f < 2; ++f)
          hv[p][k][f] = __builtin_amdgcn_raw_buffer_load_b128(rs, l * 16, hvo + (p * NFTP + 2 * k + f) * 1024, 16);
    }
    if (w < 3) {
#pragma unroll
      for (int p = 0; p < 2; ++p)
#pragma unroll
        for (int f = 0; f < 2; ++f)
          hv[p][5][f] = __builtin_amdgcn_raw_buffer_load_b128(rs, l * 16, hvo + (p * NFTP + 10 + f) * 1024, 16);
    }
    __builtin_amdgcn_sched_barrier(0);
#pragma unroll
    for (int p = 0; p < 2; ++p) {
      f32x4 acc[4][2];
#pragma unroll
      for (int a = 0; a < 4; ++a) { acc[a][0] = (f32x4){0, 0, 0, 0}; acc[a][1] = (f32x4){0, 0, 0, 0}; }
#pragma unroll
      for (int k = 0; k < 6; ++k) {
        if (k < 5 || w < 3) {
          u32x4 x0 = {hv[p][k][0][0], hv[p][k][0][1], hv[p][k][1][0], hv[p][k][1][1]};
          u32x4 x1 = {hv[p][k][0][2], hv[p][k][0][3], hv[p][k][1][2], hv[p][k][1][3]};
          bf16x8 h0 = __builtin_bit_cast(bf16x8, x0), h1 = __builtin_bit_cast(bf16x8, x1);
#pragma unroll
          for (int a = 0; a < 4; ++a) {
            acc[a][0] = __builtin_amdgcn_mfma_f32_16x16x32_bf16(w2f[k][a], h0, acc[a][0], 0, 0, 0);
            acc[a][1] = __builtin_amdgcn_mfma_f32_16x16x32_bf16(w2f[k][a], h1, acc[a][1], 0, 0, 0);
          }
        }
      }
      __builtin_amdgcn_sched_barrier(0);
#pragma unroll
      for (int a = 0; a < 4; ++a)
#pragma unroll
        for (int t = 0; t < 2; ++t) *(f32x4*)(smem + K4_LDS_R + ((w * 4 + a) * 4 + 2 * p + t) * 1024 + l * 16) = acc[a][t];
      __builtin_amdgcn_sched_barrier(0);
    }
    STAMP(3, 1);
    raw_barrier();
    STAMP(3, 2);
    const float bq[4] = {bflo(b2v[0]), bfhi(b2v[0]), bflo(b2v[1]), bfhi(b2v[1])};
#pragma unroll
    for (int i = 0; i < 2; ++i) {
      const int t = 2 * i + (w & 1);
      f32x4 sacc = *(const f32x4*)(smem + K4_LDS_R + ((0 * 4 + (w >> 1)) * 4 + t) * 1024 + l * 16);
#pragma unroll
      for (int ww = 1; ww < 8; ++ww) sacc += *(const f32x4*)(smem + K4_LDS_R + ((ww * 4 + (w >> 1)) * 4 + t) * 1024 + l * 16);
      float o[4];
      o[0] = bflo(hres[i][0]) + rbf(rbf(sacc[0]) + bq[0]);
      o[1] = bfhi(hres[i][0]) + rbf(rbf(sacc[1]) + bq[1]);
      o[2] = bflo(hres[i][1]) + rbf(rbf(sacc[2]) + bq[2]);
      o[3] = bfhi(hres[i][1]) + rbf(rbf(sacc[3]) + bq[3]);
      u32x2 pk = {pk_bf16(o[0], o[1]), pk_bf16(o[2], o[3])};
      *(u32x2*)(out + (size_t)(tb * 64 + t * 16 + l15) * DM + cg * 64 + (w >> 1) * 16 + 4 * g) = pk;
    }
  }
  STAMP(3, 3);
  STAMP_END(3, 4);
}

extern "C" void kernel_launch(void* const* d_in, const int* in_sizes, int n_in,
                              void* d_out, int out_size, void* d_ws, size_t ws_size,
                              hipStream_t stream) {
  const u16* x = (const u16*)d_in[0];
  const u16* lar = (const u16*)d_in[1];
  const u16* aim = (const u16*)d_in[2];
  const u16* cr = (const u16*)d_in[3];
  const u16* ci = (const u16*)d_in[4];
  const u16* ldt = (const u16*)d_in[5];
  const u16* dsk = (const u16*)d_in[6];
  const u16* w1 = (const u16*)d_in[8];
  const u16* b1 = (const u16*)d_in[9];
  const u16* w2 = (const u16*)d_in[10];
  const u16* b2 = (const u16*)d_in[11];
  char* ws = (char*)d_ws;
  k1_prep<<<256, 1024, 0, stream>>>(x, lar, aim, cr, ci, ldt, w1, w2, ws);
  kb_main<<<256, 512, 0, stream>>>(dsk, b1, b2, ws, (u16*)d_out);
}
```

```cpp
#include <hip/hip_runtime.h>
#include <hip/hip_bf16.h>
#include <stdint.h>

typedef unsigned short u16;
typedef __attribute__((ext_vector_type(8))) _Float16 f16x8;
typedef __attribute__((ext_vector_type(2))) _Float16 f16x2;
typedef __attribute__((ext_vector_type(8))) __bf16 bf16x8;
typedef __attribute__((ext_vector_type(2))) __bf16 bf16x2;
typedef __attribute__((ext_vector_type(2))) float f32x2;
typedef __attribute__((ext_vector_type(4))) float f32x4;
typedef __attribute__((ext_vector_type(4))) unsigned u32x4;
typedef __attribute__((ext_vector_type(2))) unsigned u32x2;
typedef __attribute__((ext_vector_type(4))) short s16x4;
#define LDS_AS __attribute__((address_space(3)))

#define SEQ 2048
#define DM 512
#define NM 128
#define FF 1376
#define NFT 86
#define NKS2 43

#define NFTP 88
#define NKSP 44
#define UT_FRONT 512
#define UT_PITCH 4736
#define XT_PITCH 4224
#define WS_SYNC 0
#define WS_UT 8192
#define KR_PITCH 4608
#define WS_KR (WS_UT + DM * UT_PITCH)
#define WS_KSC (WS_KR + DM * KR_PITCH)
#define WS_W1T (WS_KSC + DM * 4)
#define WS_W2T (WS_W1T + NFTP * 16 * 1024)
#define WS_XT (WS_W2T + 32 * NKSP * 1024)
#define WS_HT (WS_XT + DM * XT_PITCH)
#define WS_HACT (WS_HT + DM * XT_PITCH)
#define WS_END (WS_HACT + 128 * NKSP * 1024)

#define STAMP(k, i)
#define STAMP_END(k, i)

__device__ __forceinline__ float bf2f(unsigned v) { return __uint_as_float(v << 16); }
__device__ __forceinline__ float bflo(unsigned v) { return __uint_as_float(v << 16); }
__device__ __forceinline__ float bfhi(unsigned v) { return __uint_as_float(v & 0xffff0000u); }
__device__ __forceinline__ unsigned pk_bf16(float a, float b) {
  f32x2 v = {a, b};
  bf16x2 r = __builtin_convertvector(v, bf16x2);
  return __builtin_bit_cast(unsigned, r);
}
__device__ __forceinline__ unsigned pk_f16(float a, float b) {
  f32x2 v = {a, b};
  f16x2 r = __builtin_convertvector(v, f16x2);
  return __builtin_bit_cast(unsigned, r);
}
__device__ __forceinline__ float rbf(float a) { return bflo(pk_bf16(a, a)); }
__device__ __forceinline__ float h2f_lo(unsigned v) { f16x2 h = __builtin_bit_cast(f16x2, v); return (float)h[0]; }
__device__ __forceinline__ float h2f_hi(unsigned v) { f16x2 h = __builtin_bit_cast(f16x2, v); return (float)h[1]; }
__device__ __forceinline__ unsigned lds_addr(const void* p) { return (unsigned)(size_t)p; }
__device__ __forceinline__ u32x2 tr_read(unsigned ad) {
  s16x4 v = __builtin_amdgcn_ds_read_tr16_b64_v4i16((LDS_AS s16x4*)(size_t)ad);
  return __builtin_bit_cast(u32x2, v);
}
__device__ __forceinline__ void glds16(const void* gsrc, unsigned lds_dst) {
  unsigned keep;
  asm volatile("s_mov_b32 %0, m0\n\ts_mov_b32 m0, %2\n\ts_nop 0\n\tglobal_load_lds_dwordx4 %1, off\n\ts_mov_b32 m0, %0"
               : "=&s"(keep) : "v"(gsrc), "s"(lds_dst) : "memory");
}
__device__ __forceinline__ void glds16_sc1(const void* gsrc, unsigned lds_dst) {
  unsigned keep;
  asm volatile("s_mov_b32 %0, m0\n\ts_mov_b32 m0, %2\n\ts_nop 0\n\tglobal_load_lds_dwordx4 %1, off sc1\n\ts_mov_b32 m0, %0"
               : "=&s"(keep) : "v"(gsrc), "s"(lds_dst) : "memory");
}
__device__ __forceinline__ void store8_dev(void* p, u32x2 v) {
  unsigned long long x = ((unsigned long long)v[1] << 32) | v[0];
  __hip_atomic_store((unsigned long long*)p, x, __ATOMIC_RELAXED, __HIP_MEMORY_SCOPE_AGENT);
}
__device__ __forceinline__ void raw_barrier() {
  asm volatile("s_waitcnt lgkmcnt(0)" ::: "memory");
  __builtin_amdgcn_s_barrier();
  asm volatile("" ::: "memory");
}
template <int CTRL>
__device__ __forceinline__ float dpp_add(float v) {
  int r = __builtin_amdgcn_update_dpp(0, __float_as_int(v), CTRL, 0xf, 0xf, false);
  return v + __int_as_float(r);
}
__device__ __forceinline__ float row16_sum(float v) {
  v = dpp_add<0xB1>(v);
  v = dpp_add<0x4E>(v);
  v = dpp_add<0x141>(v);
  v = dpp_add<0x140>(v);
  return v;
}

struct cpx { float r, i; };
__device__ __forceinline__ cpx cmul(cpx a, cpx b) {
  cpx o; o.r = a.r * b.r - a.i * b.i; o.i = a.r * b.i + a.i * b.r; return o;
}
__device__ __forceinline__ cpx cexp_dir(float lr2, float fr, float e) {
  float mag = __builtin_amdgcn_exp2f(lr2 * e);
  float ph = __builtin_amdgcn_fractf(fr * e);
  cpx o; o.r = mag * __builtin_amdgcn_cosf(ph); o.i = mag * __builtin_amdgcn_sinf(ph); return o;
}

#define KB_CODE_LINES 96
#define K1_AROW 528
#define K1_IMG (32 * K1_AROW)
#define K1_LDS_A 0
#define K1_LDS_B (2 * K1_IMG)
#define K1_KCB 1344
#define K1_LDS_KC (4 * K1_IMG)
#define K1_LDS_RED (K1_LDS_KC + 2 * K1_KCB)
#define K1_LDS_FLAG (K1_LDS_RED + 256)
#define K1_LDS_W (K1_LDS_RED + 256 + 80)
#define K1_LDS_SIZE (K1_LDS_W + 4 * 2 * 2560)

extern "C" __global__ __launch_bounds__(1024) void k1_prep(const u16* __restrict__ x, const u16* __restrict__ lar,
                                                const u16* __restrict__ aim, const u16* __restrict__ cre,
                                                const u16* __restrict__ cim, const u16* __restrict__ ldt,
                                                const u16* __restrict__ w1, const u16* __restrict__ w2,
                                                char* __restrict__ ws) {
  __shared__ __attribute__((aligned(16))) char smem[K1_LDS_SIZE];
  const int b = blockIdx.x, tid = threadIdx.x, l = tid & 63;
  const int w = __builtin_amdgcn_readfirstlane(tid >> 6);
  const int role = w >> 2, wr = w & 3;
  const int g = l >> 4, l15 = l & 15;
  float* red = (float*)(smem + K1_LDS_RED);
  volatile LDS_AS unsigned* lnflag = (volatile LDS_AS unsigned*)(size_t)(lds_addr(smem) + K1_LDS_FLAG);
  STAMP(0, 0);

  if (role == 0) {
    const int rb = (b & 7) * 32 + (b >> 3);
    unsigned xv[8];
    float piv[8];
    const unsigned* xr = (const unsigned*)(x + rb * 8 * DM + 128 * wr + 2 * l);
#pragma unroll
    for (int r = 0; r < 8; ++r) xv[r] = xr[r * 256];
#pragma unroll
    for (int r = 0; r < 8; ++r) piv[r] = bf2f(x[(rb * 8 + r) * DM]);
    if (l == 0) lnflag[wr] = 0u;
    raw_barrier();
    const int ch = 128 * wr + 2 * l;
    {
      u32x4 x0, x1;
#pragma unroll
      for (int q = 0; q < 4; ++q) {
        x0[q] = __builtin_amdgcn_perm(xv[2 * q + 1], xv[2 * q], 0x05040100u);
        x1[q] = __builtin_amdgcn_perm(xv[2 * q + 1], xv[2 * q], 0x07060302u);
      }
      char* xp = ws + WS_XT + (size_t)ch * XT_PITCH + rb * 16;
      *(u32x4*)xp = x0;
      *(u32x4*)(xp + XT_PITCH) = x1;
    }
    float s[16];
#pragma unroll
    for (int r = 0; r < 8; ++r) {
      float a = bflo(xv[r]) - piv[r], bb = bfhi(xv[r]) - piv[r];
      s[r] = a + bb;
      s[8 + r] = a * a + bb * bb;
    }
    float t8[8], t4[4];
#pragma unroll
    for (int i = 0; i < 8; ++i) {
      auto sw = __builtin_amdgcn_permlane32_swap(__float_as_uint(s[i]), __float_as_uint(s[8 + i]), false, false);
      t8[i] = __uint_as_float(sw[0]) + __uint_as_float(sw[1]);
    }
#pragma unroll
    for (int i = 0; i < 4; ++i) {
      auto sw = __builtin_amdgcn_permlane16_swap(__float_as_uint(t8[i]), __float_as_uint(t8[4 + i]), false, false);
      t4[i] = row16_sum(__uint_as_float(sw[0]) + __uint_as_float(sw[1]));
    }
    if (l15 == 0) *(f32x4*)(red + wr * 16 + g * 4) = (f32x4){t4[0], t4[1], t4[2], t4[3]};
    asm volatile("s_waitcnt lgkmcnt(0)" ::: "memory");
    if (l == 0) lnflag[wr] = 1u;
    STAMP(0, 1);
#pragma unroll 1
    for (unsigned it = 0; it < (1u << 20); ++it) {
      unsigned f = lnflag[l & 3];
      if (__all(f == 1u)) break;
      __builtin_amdgcn_s_sleep(1);
    }
    asm volatile("" ::: "memory");
    STAMP(0, 2);
    float mb_l, rd_l;
    {
      const int r = l & 7;
      float t1 = red[r] + red[16 + r] + red[32 + r] + red[48 + r];
      float t2 = red[8 + r] + red[24 + r] + red[40 + r] + red[56 + r];
      float pv = piv[0];
#pragma unroll
      for (int k = 1; k < 8; ++k) pv = r == k ? piv[k] : pv;
      float dm = t1 * (1.f / 512.f);
      float var = fmaxf(t2 * (1.f / 512.f) - dm * dm, 0.f);
      mb_l = rbf(pv + dm);
      rd_l = __builtin_amdgcn_rcpf(rbf(__builtin_amdgcn_sqrtf(rbf(rbf(var) + 1e-5f))));
    }
    unsigned ub[8];
#pragma unroll
    for (int r = 0; r < 8; ++r) {
      const float mb = __int_as_float(__builtin_amdgcn_readlane(__float_as_int(mb_l), r));
      const float rd = __int_as_float(__builtin_amdgcn_readlane(__float_as_int(rd_l), r));
      unsigned dd = pk_bf16(bflo(xv[r]) - mb, bfhi(xv[r]) - mb);
      unsigned uu = pk_bf16(bflo(dd) * rd, bfhi(dd) * rd);
      ub[r] = pk_f16(bflo(uu), bfhi(uu));
    }
    u32x4 u0, u1;
#pragma unroll
    for (int q = 0; q < 4; ++q) {
      u0[q] = __builtin_amdgcn_perm(ub[2 * q + 1], ub[2 * q], 0x05040100u);
      u1[q] = __builtin_amdgcn_perm(ub[2 * q + 1], ub[2 * q], 0x07060302u);
    }
    char* up = ws + WS_UT + (size_t)ch * UT_PITCH + UT_FRONT + rb * 16;
    *(u32x4*)up = u0;
    *(u32x4*)(up + UT_PITCH) = u1;
    STAMP(0, 3);
    STAMP_END(0, 6);
    return;
  } else if (role == 3) {
    const int gw = b * 4 + wr;
    char* wl = smem + K1_LDS_W + wr * 5120;
    unsigned cpf0 = 0;
    if (wr == 1) {
      unsigned long long ca;
      unsigned rlo, rhi;
      asm volatile("s_getpc_b64 %0\n\ts_mov_b32 %1, k1_prep@rel32@lo+4\n\ts_mov_b32 %2, k1_prep@rel32@hi+12" : "=&s"(ca), "=&s"(rlo), "=&s"(rhi));
      ca += ((unsigned long long)rhi << 32) | rlo;
      cpf0 = *(const unsigned*)((const char*)ca + l * 128);
    }
    u32x4 v[2][2];
#pragma unroll
    for (int it = 0; it < 2; ++it) {
      const int bi = gw + it * 1024;
      if (bi < 688) {
        const int ks = bi / 43, fb = bi - ks * 43;
        const u16* src = w1 + (size_t)(32 * ks + (l >> 2)) * FF + 32 * fb + 8 * (l & 3);
        v[it][0] = *(const u32x4*)src;
        v[it][1] = *(const u32x4*)(src + 16 * FF);
      } else if (bi < 1376) {
        const int b2 = bi - 688, ks = b2 >> 4, nbk = b2 & 15;
        const u16* src = w2 + (size_t)(32 * ks + (l >> 2)) * DM + 32 * nbk + 8 * (l & 3);
        v[it][0] = *(const u32x4*)src;
        v[it][1] = *(const u32x4*)(src + 16 * DM);
      }
    }
    raw_barrier();
    if (wr == 0 && l < 40) {
#pragma unroll
      for (int cc = 0; cc < 2; ++cc)
        *(u32x4*)(ws + WS_UT + (size_t)(2 * b + cc) * UT_PITCH + (l < 32 ? l * 16 : 4608 + (l - 32) * 16)) = (u32x4){0, 0, 0, 0};
    }
    unsigned cpf = 0;
    if (wr == 3) {
      unsigned long long ca;
      unsigned rlo, rhi;
      asm volatile("s_getpc_b64 %0\n\ts_mov_b32 %1, kb_main@rel32@lo+4\n\ts_mov_b32 %2, kb_main@rel32@hi+12" : "=&s"(ca), "=&s"(rlo), "=&s"(rhi));
      ca += ((unsigned long long)rhi << 32) | rlo;
      const int li = ((b >> 3) & 1) * 64 + l;
      cpf = *(const unsigned*)((const char*)ca + (li < KB_CODE_LINES ? li : 0) * 128);
    }
    if (wr == 2) {
      if (b < 32) *(u32x4*)(ws + WS_W1T + (size_t)(86 * 16 + b) * 1024 + l * 16) = (u32x4){0, 0, 0, 0};
      else if (b < 64) *(u32x4*)(ws + WS_W2T + (size_t)((b - 32) * NKSP + 43) * 1024 + l * 16) = (u32x4){0, 0, 0, 0};
      else if (b < 72) *(u32x4*)(ws + WS_SYNC + (b - 64) * 1024 + l * 16) = (u32x4){0, 0, 0, 0};
    }
#pragma unroll
    for (int it = 0; it < 2; ++it) {
      const int bi = gw + it * 1024;
      if (bi < 1376) {
        char* t = wl + it * 2560;
        *(u32x4*)(t + (l >> 2) * 80 + (l & 3) * 16) = v[it][0];
        *(u32x4*)(t + (16 + (l >> 2)) * 80 + (l & 3) * 16) = v[it][1];
        const bool isw1 = bi < 688;
        const int q = l15 >> 2, p = l15 & 3;
        const unsigned a0 = lds_addr(t) + (isw1 ? (8 * g + q) : (4 * g + q)) * 80 + p * 8;
        const unsigned dr = isw1 ? 4 * 80 : 16 * 80;
#pragma unroll
        for (int ct = 0; ct < 2; ++ct) {
          u32x2 lo = tr_read(a0 + ct * 32), hi = tr_read(a0 + ct * 32 + dr);
          u32x4 o = {lo[0], lo[1], hi[0], hi[1]};
          if (isw1) {
            const int ks = bi / 43, fb = bi - ks * 43, ft = 2 * fb + ct;
            __builtin_nontemporal_store(o, (u32x4*)(ws + WS_W1T + (size_t)(ft * 16 + ks) * 1024 + l * 16));
          } else {
            const int b2 = bi - 688, ks = b2 >> 4, nt = 2 * (b2 & 15) + ct;
            __builtin_nontemporal_store(o, (u32x4*)(ws + WS_W2T + (size_t)(nt * NKSP + ks) * 1024 + l * 16));
          }
        }
      }
    }
    asm volatile("" :: "v"(cpf), "v"(cpf0));
    STAMP(0, 3);
    STAMP_END(0, 6);
    return;
  } else {
    const int c = role - 1, part = c ? (wr ^ 2) : wr, d = 2 * b + c;
    __builtin_amdgcn_s_setprio(2);
    const unsigned v_lar = *(const unsigned*)(lar + d * NM + 2 * l), v_aim = *(const unsigned*)(aim + d * NM + 2 * l);
    const unsigned v_cr = *(const unsigned*)(cre + d * NM + 2 * l), v_ci = *(const unsigned*)(cim + d * NM + 2 * l);
    const unsigned v_ldt = ldt[d];
    volatile LDS_AS unsigned* kf = lnflag + 4 + c * 8;
    if (l == 0) { kf[wr] = 0u; kf[4 + wr] = 0u; }
    asm volatile("" ::: "memory");
    raw_barrier();
    const float dt = rbf(__expf(bf2f(v_ldt)));
    float lr2[2], fr[2];
    lr2[0] = (-rbf(__expf(bflo(v_lar))) * dt) * 1.4426950408889634f;
    lr2[1] = (-rbf(__expf(bfhi(v_lar))) * dt) * 1.4426950408889634f;
    fr[0] = (bflo(v_aim) * dt) * 0.15915494309189535f;
    fr[1] = (bfhi(v_aim) * dt) * 0.15915494309189535f;
    if (part == 3 && l < 2) *(u32x4*)(smem + K1_LDS_KC + c * K1_KCB + l * 16) = (u32x4){0, 0, 0, 0};
    if (part < 2) {
      float cm = fmaxf(fmaxf(fabsf(bflo(v_cr)), fabsf(bfhi(v_cr))), fmaxf(fabsf(bflo(v_ci)), fabsf(bfhi(v_ci))));
#pragma unroll
      for (int o = 32; o > 0; o >>= 1) cm = fmaxf(cm, __shfl_xor(cm, o));
      const float cinv = cm > 0.f ? 1.f / cm : 0.f;
      char* Aimg = smem + K1_LDS_A + c * K1_IMG + (16 * part) * K1_AROW + l * 8;
      cpx P[2][2], z8[2];
      const float e0 = 128.f * part;
#pragma unroll
      for (int m = 0; m < 2; ++m) {
        cpx C; C.r = (m ? bfhi(v_cr) : bflo(v_cr)) * cinv; C.i = (m ? bfhi(v_ci) : bflo(v_ci)) * cinv;
        z8[m] = cexp_dir(lr2[m], fr[m], 8.f);
        P[m][0] = cmul(C, cexp_dir(lr2[m], fr[m], e0));
        P[m][1] = cmul(C, cexp_dir(lr2[m], fr[m], e0 + 64.f));
      }
#pragma unroll
      for (int a = 0; a < 8; ++a) {
#pragma unroll
        for (int k = 0; k < 2; ++k) {
          u32x2 o = {pk_f16(P[0][k].r, P[0][k].i), pk_f16(P[1][k].r, P[1][k].i)};
          *(u32x2*)(Aimg + (8 * k + a) * K1_AROW) = o;
          if (a < 7) { P[0][k] = cmul(P[0][k], z8[0]); P[1][k] = cmul(P[1][k], z8[1]); }
        }
      }
      if (part == 0 && l == 0) ((float*)(ws + WS_KSC))[d] = 2.f * dt * cm * 128.f;
    } else {
      char* Bimg = smem + K1_LDS_B + c * K1_IMG + l * 8;
#define PUTB(col, v0, v1) { cpx _a = (v0), _b = (v1); u32x2 _o = {pk_f16(_a.r, -_a.i), pk_f16(_b.r, -_b.i)}; *(u32x2*)(Bimg + (col) * K1_AROW) = _o; }
      if (part == 2) {
        cpx z[2][8], e256[2];
#pragma unroll
        for (int m = 0; m < 2; ++m) {
          z[m][0].r = 1.f; z[m][0].i = 0.f;
          z[m][1] = cexp_dir(lr2[m], fr[m], 1.f);
          z[m][2] = cmul(z[m][1], z[m][1]); z[m][3] = cmul(z[m][2], z[m][1]); z[m][4] = cmul(z[m][2], z[m][2]);
          z[m][5] = cmul(z[m][4], z[m][1]); z[m][6] = cmul(z[m][4], z[m][2]); z[m][7] = cmul(z[m][4], z[m][3]);
          e256[m] = cexp_dir(lr2[m], fr[m], 256.f);
        }
#pragma unroll
        for (int k = 0; k < 8; ++k) PUTB(k, z[0][k], z[1][k]);
        PUTB(8, e256[0], e256[1]);
#pragma unroll
        for (int k = 1; k < 4; ++k) PUTB(8 + k, cmul(e256[0], z[0][2 * k]), cmul(e256[1], z[1][2 * k]));
      } else {
        cpx z4[2], e256[2], e512[2], e768[2], e1024[2];
#pragma unroll
        for (int m = 0; m < 2; ++m) {
          z4[m] = cexp_dir(lr2[m], fr[m], 4.f);
          e256[m] = cexp_dir(lr2[m], fr[m], 256.f);
          e1024[m] = cexp_dir(lr2[m], fr[m], 1024.f);
          e512[m] = cmul(e256[m], e256[m]); e768[m] = cmul(e512[m], e256[m]);
        }
        PUTB(12, e512[0], e512[1]); PUTB(13, cmul(e512[0], z4[0]), cmul(e512[1], z4[1]));
        PUTB(14, e768[0], e768[1]); PUTB(15, cmul(e768[0], z4[0]), cmul(e768[1], z4[1]));
        PUTB(16, e1024[0], e1024[1]);
        PUTB(17, cmul(e1024[0], e256[0]), cmul(e1024[1], e256[1]));
        PUTB(18, cmul(e1024[0], e512[0]), cmul(e1024[1], e512[1]));
        PUTB(19, cmul(e1024[0], e768[0]), cmul(e1024[1], e768[1]));
        PUTB(20, cmul(e1024[0], e1024[0]), cmul(e1024[1], e1024[1]));
      }
#undef PUTB
    }
    STAMP(0, 1);
    asm volatile("s_waitcnt lgkmcnt(0)" ::: "memory");
    if (l == 0) kf[wr] = 1u;
#pragma unroll 1
    for (unsigned it = 0; it < (1u << 20); ++it) {
      unsigned f = kf[l & 3];
      if (__all(f == 1u)) break;
      __builtin_amdgcn_s_sleep(1);
    }
    asm volatile("" ::: "memory");
    STAMP(0, 2);
    {
      const int rt = wr >> 1, ct = wr & 1;
      const char* Ab = smem + K1_LDS_A + c * K1_IMG + (16 * rt + l15) * K1_AROW + g * 16;
      const int bc = 16 * ct + l15;
      const char* Bb = smem + K1_LDS_B + c * K1_IMG + (bc < 21 ? bc : 20) * K1_AROW + g * 16;
      f16x8 af[8], bfr[8];
#pragma unroll
      for (int q = 0; q < 8; ++q) { af[q] = *(const f16x8*)(Ab + q * 64); bfr[q] = *(const f16x8*)(Bb + q * 64); }
      f32x4 acc = {0, 0, 0, 0};
#pragma unroll
      for (int q = 0; q < 8; ++q) acc = __builtin_amdgcn_mfma_f32_16x16x32_f16(af[q], bfr[q], acc, 0, 0, 0);
      _Float16* kc = (_Float16*)(smem + K1_LDS_KC + c * K1_KCB + 32);
      const int a0 = 16 * rt + 4 * g;
      int base, stride;
      if (ct == 0) {
        if (l15 < 8) { base = l15; stride = 8; }
        else if (l15 < 12) { base = 256 + (l15 - 8); stride = 4; }
        else { base = 384 + 64 * ((l15 - 12) >> 1) + (l15 & 1); stride = 2; }
      } else {
        base = 512 + 32 * l15; stride = 1;
      }
#pragma unroll
      for (int r = 0; r < 4; ++r) {
        const bool ok = ct == 0 || l15 < 4 || (l15 == 4 && a0 + r == 0);
        if (ok) kc[base + stride * (a0 + r)] = (_Float16)(acc[r] * (1.f / 128.f));
      }
    }
    STAMP(0, 3);
    asm volatile("s_waitcnt lgkmcnt(0)" ::: "memory");
    if (l == 0) kf[4 + wr] = 1u;
    if (wr < 2) {
#pragma unroll 1
      for (unsigned it = 0; it < (1u << 20); ++it) {
        unsigned f = kf[4 + (l & 3)];
        if (__all(f == 1u)) break;
        __builtin_amdgcn_s_sleep(1);
      }
      asm volatile("" ::: "memory");
      STAMP(0, 4);
      const int ci = wr * 64 + l;
      if (ci < 84) *(u32x4*)(ws + WS_KR + (size_t)d * K1_KCB + ci * 16) = *(const u32x4*)(smem + K1_LDS_KC + c * K1_KCB + ci * 16);
    }
  }
  STAMP(0, 5);
  STAMP_END(0, 6);
}

#define K2_ROWB 272
#define K2_COPY (18 * K2_ROWB)
#define K2_UCH (4 * K2_COPY)
#define K2_LDS_U 0
#define K2_LDS_K (39 * 1024)
#define K2_LDS_X (K2_LDS_K + 2 * 4608)
#define KG_KCB 1344
#define KG_LDS_KC (K2_LDS_X + 8192)
#define K2_LDS_Y (KG_LDS_KC + 3072)
#define K3_LDS_H 0
#define K3_LDS_E 65536
#define K3_LDS_ST (K3_LDS_E + 8 * 9216)
#define K4_LDS_R 0
#define KB_LDS_SIZE (K3_LDS_ST + 1024)
#define SPIN_MAX (1u << 22)

__device__ __forceinline__ f16x8 ld_g(const char* p) {
  u32x2 lo = *(const u32x2*)p, hi = *(const u32x2*)(p + 8);
  u32x4 t = {lo[0], lo[1], hi[0], hi[1]};
  return __builtin_bit_cast(f16x8, t);
}
__device__ __forceinline__ bf16x8 ld_bfrag(unsigned ad) {
  u32x2 l2 = tr_read(ad), h2 = tr_read(ad + 512);
  u32x4 tt = {l2[0], l2[1], h2[0], h2[1]};
  return __builtin_bit_cast(bf16x8, tt);
}

extern "C" __global__ __launch_bounds__(512, 2) void kb_main(const u16* __restrict__ dsk,
                                               const u16* __restrict__ b1, const u16* __restrict__ b2,
                                               char* __restrict__ ws, u16* __restrict__ out) {
  __shared__ __attribute__((aligned(16))) char smem[KB_LDS_SIZE];
  const int b = blockIdx.x, tid = threadIdx.x, l = tid & 63;
  const int w = __builtin_amdgcn_readfirstlane(tid >> 6);
  const int g = l >> 4, l15 = l & 15;
  const int cp = b;
  const int tb = (b & 7) * 4 + ((b >> 3) & 3);
  const int cg = b >> 5;
  const int wl = w & 3, wh = w >> 2;
  const unsigned sb = lds_addr(smem);
  unsigned* sync = (unsigned*)(ws + WS_SYNC);
  STAMP(1, 0);
  if (w < 3) {
    const int o = w * 1024 + l * 16;
    glds16(ws + WS_KR + (size_t)cp * 2 * KG_KCB + (o < 2688 ? o : 0), sb + KG_LDS_KC + w * 1024);
  }
  {
    const char* ub = ws + WS_UT + (size_t)(2 * cp + wh) * UT_PITCH + wl * 2;
    const unsigned ud = sb + K2_LDS_U + w * K2_COPY;
#pragma unroll
    for (int it = 0; it < 5; ++it) {
      const int i = it * 64 + l;
      const int row = (i * 241) >> 12, q = i - row * 17;
      if (it < 4 || l < 50) glds16(ub + (128 * row + 8 * q) * 2, ud + it * 1024);
    }
    glds16(ws + WS_XT + (size_t)(2 * cp + wh) * XT_PITCH + wl * 1024 + l * 16, sb + K2_LDS_X + w * 1024);
  }
  const int myft = cg * 11 + 3 * wl;
  bf16x8 wf[3][8];
  const __amdgpu_buffer_rsrc_t rw1 = __builtin_amdgcn_make_buffer_rsrc(ws + WS_W1T, 0, NFTP * 16 * 1024, 0x00020000);
  const unsigned w1o = (myft * 16 + wh * 8) * 1024;
  const unsigned w1o2 = w1o + (wl < 3 ? 2 : 1) * 16 * 1024;
#define LDW1(a, k) __builtin_bit_cast(bf16x8, __builtin_amdgcn_raw_buffer_load_b128(rw1, l * 16, ((a) == 2 ? w1o2 : w1o + (a) * 16 * 1024) + (k) * 1024, 0))
  if (wl == 3 && l < 18) *(u32x4*)(smem + K2_LDS_K + wh * 4608 + (l < 3 ? l : 257 + l) * 16) = (u32x4){0u, 0u, 0u, 0u};
  asm volatile("s_waitcnt vmcnt(6)" ::: "memory");
  raw_barrier();
  STAMP(1, 8);
  const float ksc = ((const float*)(ws + WS_KSC))[2 * cp + wh];
  const unsigned dkv = *(const unsigned*)(dsk + 2 * cp);
  {
    const int c2 = (tid & 255) + 1;
    int ih; unsigned tbl;
    if (c2 <= 32) { ih = 8 * c2; tbl = 0x76543210u; }
    else if (c2 <= 64) { ih = 256 + 4 * (c2 - 32); tbl = 0x43222100u; }
    else if (c2 <= 128) { ih = 384 + 2 * (c2 - 64); tbl = 0x22111000u; }
    else { ih = 512 + (c2 - 128); tbl = 0x11100000u | ((unsigned)(c2 & 1) << 16); }
    const u16* kc0 = (const u16*)(smem + KG_LDS_KC + wh * KG_KCB + 32);
    const u16* kc = kc0 + ih;
    unsigned v[8];
#pragma unroll
    for (int j = 0; j < 8; ++j) v[j] = kc[-(int)((tbl >> (4 * j)) & 15u)];
    if (c2 == 256) v[0] = 0u;
    u32x4 o = {v[0] | (v[1] << 16), v[2] | (v[3] << 16), v[4] | (v[5] << 16), v[6] | (v[7] << 16)};
    *(u32x4*)(smem + K2_LDS_K + wh * 4608 + (259 - c2) * 16) = o;
    if (c2 == 1) *(u32x4*)(smem + K2_LDS_K + wh * 4608 + 259 * 16) = (u32x4){(unsigned)kc0[0], 0u, 0u, 0u};
  }
  STAMP(1, 7);
  asm volatile("s_waitcnt vmcnt(0)" ::: "memory");
  raw_barrier();
  STAMP(1, 1);
  const u32x2 b2v = *(const u32x2*)(b2 + cg * 64 + (w >> 1) * 16 + 4 * g);
  u32x2 b1v[3];
#pragma unroll
  for (int a = 0; a < 3; ++a) {
    const int f = 16 * (myft + a) + 4 * g;
    b1v[a] = *(const u32x2*)(b1 + (f < FF ? f : 0));
  }
  {
    f32x4 acc[8];
#pragma unroll
    for (int i = 0; i < 8; ++i) acc[i] = (f32x4){0, 0, 0, 0};
    const int phi = l15 & 7, nu = l15 >> 3;
    const char* Fb = smem + K2_LDS_K + wh * 4608 + (259 - l15 + g) * 16 + 192 - 64 * wl;
    const char* Gb = smem + K2_LDS_U + wh * K2_UCH + (phi & 3) * K2_COPY + (nu + 2) * K2_ROWB + ((phi & 4) + 8 * g) * 2 + 192 - 64 * wl;
#pragma unroll
    for (int rr = 0; rr < 2; ++rr) {
      f16x8 F[8], G[8];
#pragma unroll
      for (int q = 0; q < 8; ++q) { G[q] = ld_g(Gb - rr * K2_ROWB + q * 2 * K2_ROWB); F[q] = *(const f16x8*)(Fb - rr * 256 - q * 512); }
      __builtin_amdgcn_sched_barrier(0);
#pragma unroll
      for (int k = 0; k < 8; ++k) wf[rr][k] = LDW1(rr, k);
#pragma unroll
      for (int e = 0; e < 8; ++e)
#pragma unroll
        for (int q = 0; q + e < 8; ++q)
          acc[q + e] = __builtin_amdgcn_mfma_f32_16x16x32_f16(G[e], F[q], acc[q + e], 0, 0, 0);
#pragma unroll
      for (int k = 0; k < 8; ++k) {
        __builtin_amdgcn_sched_group_barrier(0x008, 4, 0);
        __builtin_amdgcn_sched_group_barrier(0x020, 1, 0);
      }
      __builtin_amdgcn_sched_barrier(0);
      if (rr == 0 && wl == 0) {
        f16x8 Gm = ld_g(Gb - 2 * K2_ROWB), F8 = *(const f16x8*)(Fb - 8 * 512);
#pragma unroll
        for (int i = 0; i < 7; ++i) acc[i] = __builtin_amdgcn_mfma_f32_16x16x32_f16(Gm, F[i + 1], acc[i], 0, 0, 0);
        acc[7] = __builtin_amdgcn_mfma_f32_16x16x32_f16(Gm, F8, acc[7], 0, 0, 0);
      }
    }
    STAMP(1, 2);
#pragma unroll
    for (int i = 0; i < 8; ++i) *(f32x4*)(smem + K2_LDS_Y + (w * 8 + i) * 1024 + l * 16) = acc[i];
    raw_barrier();
    const float dk = wh ? bfhi(dkv) : bflo(dkv);
    const int tl = 128 * (g >> 1) + 8 * l15 + 4 * (g & 1);
#pragma unroll
    for (int i = 0; i < 2; ++i) {
      const int ti = 2 * wl + i;
      f32x4 o = *(const f32x4*)(smem + K2_LDS_Y + ((wh * 4 + 0) * 8 + ti) * 1024 + l * 16);
#pragma unroll
      for (int k = 1; k < 4; ++k) o += *(const f32x4*)(smem + K2_LDS_Y + ((wh * 4 + k) * 8 + ti) * 1024 + l * 16);
      const int t0 = 256 * ti + tl;
      u32x2 uu = *(const u32x2*)(smem + K2_LDS_U + wh * K2_UCH + ((t0 >> 7) + 2) * K2_ROWB + (t0 & 127) * 2);
      u32x2 xx = *(const u32x2*)(smem + K2_LDS_X + wh * 4096 + t0 * 2);
      u32x2 res;
#pragma unroll
      for (int j = 0; j < 2; ++j) {
        unsigned yy = pk_bf16(o[2 * j] * ksc, o[2 * j + 1] * ksc);
        unsigned ud = pk_bf16(h2f_lo(uu[j]) * dk, h2f_hi(uu[j]) * dk);
        unsigned z = pk_bf16(bflo(yy) + bflo(ud), bfhi(yy) + bfhi(ud));
        res[j] = pk_bf16(bflo(xx[j]) + bflo(z), bfhi(xx[j]) + bfhi(z));
      }
      store8_dev(ws + WS_HT + (size_t)(2 * cp + wh) * XT_PITCH + t0 * 2, res);
    }
  }
  STAMP(1, 3);
  asm volatile("s_waitcnt vmcnt(0)" ::: "memory");
  raw_barrier();
  if (tid == 192) __hip_atomic_fetch_add(sync + (b & 7) * 32, 1u, __ATOMIC_RELAXED, __HIP_MEMORY_SCOPE_AGENT);
  STAMP(1, 4);
  const int ks0 = w * 5 + (w < 3 ? w : 3);
  bf16x8 w2f[6][4];
  const __amdgpu_buffer_rsrc_t rw2 = __builtin_amdgcn_make_buffer_rsrc(ws + WS_W2T, 0, 32 * NKSP * 1024, 0x00020000);
  const unsigned w2o = ((cg * 4) * NKSP + ks0) * 1024;
#define LDW2(k, a) __builtin_bit_cast(bf16x8, __builtin_amdgcn_raw_buffer_load_b128(rw2, l * 16, w2o + ((a) * NKSP + (k)) * 1024, 0))
  if (wl < 3) {
#pragma unroll
    for (int k = 0; k < 8; ++k) wf[2][k] = LDW1(2, k);
  } else {
#pragma unroll
    for (int k = 0; k < 8; ++k) wf[2][k] = wf[1][k];
  }
  __builtin_amdgcn_sched_barrier(0);
  const u32x4 onesu = {0x3f803f80u, 0x3f803f80u, 0x3f803f80u, 0x3f803f80u};
  const bf16x8 onesf = __builtin_bit_cast(bf16x8, onesu);
  {
    f32x4 cq[2];
#pragma unroll
    for (int a = 0; a < 2; ++a) cq[a] = (f32x4){0, 0, 0, 0};
#pragma unroll
    for (int k = 0; k < 8; ++k)
#pragma unroll
      for (int a = 0; a < 2; ++a) cq[a] = __builtin_amdgcn_mfma_f32_16x16x32_bf16(wf[a][k], onesf, cq[a], 0, 0, 0);
#pragma unroll
    for (int a = 0; a < 2; ++a) *(f32x4*)(smem + K3_LDS_E + (w * 9 + 6 + a) * 1024 + l * 16) = cq[a];
  }
  STAMP(1, 5);
  if (w == 3) {
#pragma unroll 1
    for (unsigned it = 0; it < SPIN_MAX; ++it) {
      unsigned v = __hip_atomic_load(sync + (l & 7) * 32, __ATOMIC_RELAXED, __HIP_MEMORY_SCOPE_AGENT);
      if (__all(v >= 32u)) break;
      __builtin_amdgcn_s_sleep(1);
    }
  }
  raw_barrier();
  STAMP(2, 0);
  asm volatile("" : "+v"(wf[2][0]), "+v"(wf[2][1]), "+v"(wf[2][2]), "+v"(wf[2][3]), "+v"(wf[2][4]), "+v"(wf[2][5]), "+v"(wf[2][6]), "+v"(wf[2][7]) : "v"(b2v), "v"(b1v[0]), "v"(b1v[1]), "v"(b1v[2]) : "memory");
#pragma unroll
  for (int it = 0; it < 8; ++it) {
    const int j = it * 8 + w;
    const int row = j * 8 + (l >> 3), pc = l & 7;
    const int swz = ((row >> 1) & 1) | (((row >> 3) & 1) << 1);
    const int lc = (((pc >> 1) ^ swz) << 1) | (pc & 1);
    glds16_sc1(ws + WS_HT + (size_t)row * XT_PITCH + tb * 128 + lc * 16, sb + K3_LDS_H + j * 1024);
  }
  {
    f32x4 cq = {0, 0, 0, 0};
#pragma unroll
    for (int k = 0; k < 8; ++k) cq = __builtin_amdgcn_mfma_f32_16x16x32_bf16(wf[2][k], onesf, cq, 0, 0, 0);
    *(f32x4*)(smem + K3_LDS_E + (w * 9 + 8) * 1024 + l * 16) = cq;
  }
  asm volatile("s_waitcnt vmcnt(0)" ::: "memory");
  raw_barrier();
  STAMP(2, 1);
  const int q4 = l15 >> 2, p4 = l15 & 3;
  const __amdgpu_buffer_rsrc_t rs = __builtin_amdgcn_make_buffer_rsrc(ws + WS_HACT, 0, 64 * NFTP * 1024, 0x00020000);
  unsigned* pcA = sync + 256 + tb * 32;
  u32x2 hres[2];
  {
    const int swz = ((q4 >> 1) & 1) | ((g & 1) << 1);
    const unsigned lbase = sb + K3_LDS_H + wh * 8 * 4096 + (8 * g + q4) * 128 + p4 * 8;
    unsigned ad[4];
#pragma unroll
    for (int t = 0; t < 4; ++t) ad[t] = lbase + (((t ^ (2 * wh)) ^ swz) << 5);
    const unsigned sa = lbase + ((wl ^ swz) << 5);
    const unsigned vo2 = wl < 3 ? (unsigned)(l * 16) : 0x7f000000u;
    f32x4 acc[3][2];
    bf16x8 bf[2][2];
    auto finish = [&](int p) {
      f32x4 cs[3], bb[3];
      f32x2 st[2];
#pragma unroll
      for (int a = 0; a < 3; ++a) {
        bb[a] = (f32x4){bflo(b1v[a][0]), bfhi(b1v[a][0]), bflo(b1v[a][1]), bfhi(b1v[a][1])};
        cs[a] = *(const f32x4*)(smem + K3_LDS_E + (w * 9 + 6 + a) * 1024 + l * 16) + *(const f32x4*)(smem + K3_LDS_E + ((w ^ 4) * 9 + 6 + a) * 1024 + l * 16);
      }
#pragma unroll
      for (int t = 0; t < 2; ++t) {
        const int tr = (2 * p + t) * 16 + l15;
        f32x2 p0 = *(const f32x2*)(smem + K3_LDS_ST + tr * 8), p1 = *(const f32x2*)(smem + K3_LDS_ST + (64 + tr) * 8);
        float mean = (p0[0] + p1[0]) * (1.f / 512.f);
        float var = fmaxf((p0[1] + p1[1]) * (1.f / 512.f) - mean * mean, 0.f);
        float m1 = rbf(mean);
        float r1 = __builtin_amdgcn_rcpf(rbf(__builtin_amdgcn_sqrtf(rbf(rbf(var) + 1e-5f))));
        float mean2 = (mean - m1) * r1, var2 = var * r1 * r1;
        float m2 = rbf(mean2);
        float r2 = __builtin_amdgcn_rcpf(rbf(__builtin_amdgcn_sqrtf(rbf(rbf(var2) + 1e-5f))));
        st[t] = (f32x2){r1 * r2, -(m1 * r1 + m2) * r2};
      }
#pragma unroll
      for (int a = 0; a < 3; ++a)
#pragma unroll
        for (int t = 0; t < 2; ++t) acc[a][t] += *(const f32x4*)(smem + K3_LDS_E + ((w ^ 4) * 9 + a * 2 + t) * 1024 + l * 16);
      const unsigned so = ((tb * 2 + p) * NFTP + myft) * 1024;
      const f32x2 c1 = {-0.10294324f, -0.10294324f}, c0 = {-2.30220819f, -2.30220819f}, one = {1.f, 1.f};
      f32x2 v[12], q[12];
#pragma unroll
      for (int i = 0; i < 12; ++i) {
        const int a = i >> 2, t = (i >> 1) & 1, r = 2 * (i & 1);
        const f32x2 a2 = {st[t][0], st[t][0]}, b2 = {st[t][1], st[t][1]};
        v[i] = __builtin_elementwise_fma(a2, (f32x2){acc[a][t][r], acc[a][t][r + 1]},
                                         __builtin_elementwise_fma(b2, (f32x2){cs[a][r], cs[a][r + 1]}, (f32x2){bb[a][r], bb[a][r + 1]}));
      }
      __builtin_amdgcn_sched_barrier(0);
#pragma unroll
      for (int i = 0; i < 12; ++i) q[i] = v[i] * v[i];
      __builtin_amdgcn_sched_barrier(0);
#pragma unroll
      for (int i = 0; i < 12; ++i) q[i] = __builtin_elementwise_fma(q[i], c1, c0);
      __builtin_amdgcn_sched_barrier(0);
#pragma unroll
      for (int i = 0; i < 12; ++i) q[i] = q[i] * v[i];
      __builtin_amdgcn_sched_barrier(0);
#pragma unroll
      for (int i = 0; i < 12; ++i) q[i] = (f32x2){__builtin_amdgcn_exp2f(q[i][0]), __builtin_amdgcn_exp2f(q[i][1])};
      __builtin_amdgcn_sched_barrier(0);
#pragma unroll
      for (int i = 0; i < 12; ++i) q[i] = q[i] + one;
      __builtin_amdgcn_sched_barrier(0);
#pragma unroll
      for (int i = 0; i < 12; ++i) q[i] = (f32x2){__builtin_amdgcn_rcpf(q[i][0]), __builtin_amdgcn_rcpf(q[i][1])};
      __builtin_amdgcn_sched_barrier(0);
#pragma unroll
      for (int i = 0; i < 12; ++i) v[i] = v[i] * q[i];
      __builtin_amdgcn_sched_barrier(0);
#pragma unroll
      for (int a = 0; a < 3; ++a) {
        u32x4 o = {pk_bf16(v[4 * a][0], v[4 * a][1]), pk_bf16(v[4 * a + 1][0], v[4 * a + 1][1]),
                   pk_bf16(v[4 * a + 2][0], v[4 * a + 2][1]), pk_bf16(v[4 * a + 3][0], v[4 * a + 3][1])};
        __builtin_amdgcn_raw_buffer_store_b128(o, rs, (a < 2 ? (unsigned)(l * 16) : vo2) + so + a * 1024, 0, 16);
      }
      __builtin_amdgcn_sched_barrier(0);
    };
    {
      f32x4 sS = {0, 0, 0, 0}, sQ = {0, 0, 0, 0};
      f32x4 acs[3][2];
      bf16x8 bg[2][2], sf[2];
#pragma unroll
      for (int a = 0; a < 3; ++a)
#pragma unroll
        for (int t = 0; t < 2; ++t) { acc[a][t] = (f32x4){0, 0, 0, 0}; acs[a][t] = (f32x4){0, 0, 0, 0}; }
      bf[0][0] = ld_bfrag(ad[0]); bf[0][1] = ld_bfrag(ad[1]); bg[0][0] = ld_bfrag(ad[2]); bg[0][1] = ld_bfrag(ad[3]); sf[0] = ld_bfrag(sa);
      __builtin_amdgcn_sched_barrier(0);
#pragma unroll
      for (int ks = 0; ks < 8; ++ks) {
        const int cur = ks & 1, nx = cur ^ 1;
        if (ks < 7) {
          bf[nx][0] = ld_bfrag(ad[0] + (ks + 1) * 4096); bf[nx][1] = ld_bfrag(ad[1] + (ks + 1) * 4096);
          bg[nx][0] = ld_bfrag(ad[2] + (ks + 1) * 4096); bg[nx][1] = ld_bfrag(ad[3] + (ks + 1) * 4096);
          sf[nx] = ld_bfrag(sa + (ks + 1) * 4096);
        }
        w2f[ks >> 1][(ks & 1) * 2] = LDW2(ks >> 1, (ks & 1) * 2);
        w2f[ks >> 1][(ks & 1) * 2 + 1] = LDW2(ks >> 1, (ks & 1) * 2 + 1);
#pragma unroll
        for (int t = 0; t < 2; ++t)
#pragma unroll
          for (int a = 0; a < 3; ++a) acs[a][t] = __builtin_amdgcn_mfma_f32_16x16x32_bf16(wf[a][ks], bg[cur][t], acs[a][t], 0, 0, 0);
#pragma unroll
        for (int t = 0; t < 2; ++t)
#pragma unroll
          for (int a = 0; a < 3; ++a) acc[a][t] = __builtin_amdgcn_mfma_f32_16x16x32_bf16(wf[a][ks], bf[cur][t], acc[a][t], 0, 0, 0);
        sS = __builtin_amdgcn_mfma_f32_16x16x32_bf16(onesf, sf[cur], sS, 0, 0, 0);
        sQ = __builtin_amdgcn_mfma_f32_16x16x32_bf16(sf[cur], sf[cur], sQ, 0, 0, 0);
#pragma unroll
        for (int k = 0; k < 10; ++k) {
          __builtin_amdgcn_sched_group_barrier(0x008, 1, 0);
          if (ks < 7) __builtin_amdgcn_sched_group_barrier(0x100, 1, 0);
          if (k == 0 || k == 6) __builtin_amdgcn_sched_group_barrier(0x020, 1, 0);
        }
        __builtin_amdgcn_sched_barrier(0);
      }
      STAMP(2, 2);
#pragma unroll
      for (int a = 0; a < 3; ++a)
#pragma unroll
        for (int t = 0; t < 2; ++t) *(f32x4*)(smem + K3_LDS_E + (w * 9 + a * 2 + t) * 1024 + l * 16) = acs[a][t];
      float dq = (l15 & 2) ? ((l15 & 1) ? sQ[3] : sQ[2]) : ((l15 & 1) ? sQ[1] : sQ[0]);
      float s2 = __shfl(dq, l15 + 16 * (l15 >> 2));
      if (l < 16) *(f32x2*)(smem + K3_LDS_ST + (wh * 64 + wl * 16 + l) * 8) = (f32x2){sS[0], s2};
    }
#pragma unroll
    for (int a = 0; a < 4; ++a) w2f[4][a] = LDW2(4, a);
    if (w < 3) {
#pragma unroll
      for (int a = 0; a < 4; ++a) w2f[5][a] = LDW2(5, a);
    }
    {
      const int row = cg * 64 + (w >> 1) * 16 + 4 * g + q4;
      const int swz2 = ((row >> 1) & 1) | (((row >> 3) & 1) << 1);
      const unsigned hb = sb + K3_LDS_H + row * 128 + p4 * 8;
#pragma unroll
      for (int i = 0; i < 2; ++i) hres[i] = tr_read(hb + (((2 * i + (w & 1)) ^ swz2) << 5));
    }
    raw_barrier();
    STAMP(2, 3);
    finish(wh);
  }
  STAMP(2, 4);
  asm volatile("s_waitcnt vmcnt(0)" ::: "memory");
  raw_barrier();
  STAMP(2, 5);
  if (w == 0) {
    if (l == 0) __hip_atomic_fetch_add(pcA, 1u, __ATOMIC_RELAXED, __HIP_MEMORY_SCOPE_AGENT);
#pragma unroll 1
    for (unsigned it = 0; it < SPIN_MAX; ++it) {
      unsigned v = __hip_atomic_load(pcA, __ATOMIC_RELAXED, __HIP_MEMORY_SCOPE_AGENT);
      if (v >= 8u) break;
      __builtin_amdgcn_s_sleep(1);
    }
  }
  raw_barrier();
  STAMP(3, 0);
  {
    const unsigned hvo = ((tb * 2) * NFTP + ks0 * 2) * 1024;
    u32x4 hv[2][6][2];
#pragma unroll
    for (int p = 0; p < 2; ++p) {
#pragma unroll
      for (int k = 0; k < 5; ++k)
#pragma unroll
        for (int f = 0; f < 2; ++f)
          hv[p][k][f] = __builtin_amdgcn_raw_buffer_load_b128(rs, l * 16, hvo + (p * NFTP + 2 * k + f) * 1024, 16);
    }
    if (w < 3) {
#pragma unroll
      for (int p = 0; p < 2; ++p)
#pragma unroll
        for (int f = 0; f < 2; ++f)
          hv[p][5][f] = __builtin_amdgcn_raw_buffer_load_b128(rs, l * 16, hvo + (p * NFTP + 10 + f) * 1024, 16);
    }
    __builtin_amdgcn_sched_barrier(0);
#pragma unroll
    for (int p = 0; p < 2; ++p) {
      f32x4 acc[4][2];
#pragma unroll
      for (int a = 0; a < 4; ++a) { acc[a][0] = (f32x4){0, 0, 0, 0}; acc[a][1] = (f32x4){0, 0, 0, 0}; }
#pragma unroll
      for (int k = 0; k < 6; ++k) {
        if (k < 5 || w < 3) {
          u32x4 x0 = {hv[p][k][0][0], hv[p][k][0][1], hv[p][k][1][0], hv[p][k][1][1]};
          u32x4 x1 = {hv[p][k][0][2], hv[p][k][0][3], hv[p][k][1][2], hv[p][k][1][3]};
          bf16x8 h0 = __builtin_bit_cast(bf16x8, x0), h1 = __builtin_bit_cast(bf16x8, x1);
#pragma unroll
          for (int a = 0; a < 4; ++a) {
            acc[a][0] = __builtin_amdgcn_mfma_f32_16x16x32_bf16(w2f[k][a], h0, acc[a][0], 0, 0, 0);
            acc[a][1] = __builtin_amdgcn_mfma_f32_16x16x32_bf16(w2f[k][a], h1, acc[a][1], 0, 0, 0);
          }
        }
      }
      __builtin_amdgcn_sched_barrier(0);
#pragma unroll
      for (int a = 0; a < 4; ++a)
#pragma unroll
        for (int t = 0; t < 2; ++t) *(f32x4*)(smem + K4_LDS_R + ((w * 4 + a) * 4 + 2 * p + t) * 1024 + l * 16) = acc[a][t];
      __builtin_amdgcn_sched_barrier(0);
    }
    STAMP(3, 1);
    raw_barrier();
    STAMP(3, 2);
    const float bq[4] = {bflo(b2v[0]), bfhi(b2v[0]), bflo(b2v[1]), bfhi(b2v[1])};
#pragma unroll
    for (int i = 0; i < 2; ++i) {
      const int t = 2 * i + (w & 1);
      f32x4 sacc = *(const f32x4*)(smem + K4_LDS_R + ((0 * 4 + (w >> 1)) * 4 + t) * 1024 + l * 16);
#pragma unroll
      for (int ww = 1; ww < 8; ++ww) sacc += *(const f32x4*)(smem + K4_LDS_R + ((ww * 4 + (w >> 1)) * 4 + t) * 1024 + l * 16);
      float o[4];
      o[0] = bflo(hres[i][0]) + rbf(rbf(sacc[0]) + bq[0]);
      o[1] = bfhi(hres[i][0]) + rbf(rbf(sacc[1]) + bq[1]);
      o[2] = bflo(hres[i][1]) + rbf(rbf(sacc[2]) + bq[2]);
      o[3] = bfhi(hres[i][1]) + rbf(rbf(sacc[3]) + bq[3]);
      u32x2 pk = {pk_bf16(o[0], o[1]), pk_bf16(o[2], o[3])};
      *(u32x2*)(out + (size_t)(tb * 64 + t * 16 + l15) * DM + cg * 64 + (w >> 1) * 16 + 4 * g) = pk;
    }
  }
  STAMP(3, 3);
  STAMP_END(3, 4);
}

extern "C" void kernel_launch(void* const* d_in, const int* in_sizes, int n_in,
                              void* d_out, int out_size, void* d_ws, size_t ws_size,
                              hipStream_t stream) {
  const u16* x = (const u16*)d_in[0];
  const u16* lar = (const u16*)d_in[1];
  const u16* aim = (const u16*)d_in[2];
  const u16* cr = (const u16*)d_in[3];
  const u16* ci = (const u16*)d_in[4];
  const u16* ldt = (const u16*)d_in[5];
  const u16* dsk = (const u16*)d_in[6];
  const u16* w1 = (const u16*)d_in[8];
  const u16* b1 = (const u16*)d_in[9];
  const u16* w2 = (const u16*)d_in[10];
  const u16* b2 = (const u16*)d_in[11];
  char* ws = (char*)d_ws;
  k1_prep<<<256, 1024, 0, stream>>>(x, lar, aim, cr, ci, ldt, w1, w2, ws);
  kb_main<<<256, 512, 0, stream>>>(dsk, b1, b2, ws, (u16*)d_out);
}
```

```cpp
#include <hip/hip_runtime.h>
#include <hip/hip_bf16.h>
#include <stdint.h>

typedef unsigned short u16;
typedef __attribute__((ext_vector_type(8))) _Float16 f16x8;
typedef __attribute__((ext_vector_type(2))) _Float16 f16x2;
typedef __attribute__((ext_vector_type(8))) __bf16 bf16x8;
typedef __attribute__((ext_vector_type(2))) __bf16 bf16x2;
typedef __attribute__((ext_vector_type(2))) float f32x2;
typedef __attribute__((ext_vector_type(4))) float f32x4;
typedef __attribute__((ext_vector_type(4))) unsigned u32x4;
typedef __attribute__((ext_vector_type(2))) unsigned u32x2;
typedef __attribute__((ext_vector_type(4))) short s16x4;
#define LDS_AS __attribute__((address_space(3)))

#define SEQ 2048
#define DM 512
#define NM 128
#define FF 1376
#define NFT 86
#define NKS2 43

#define NFTP 88
#define NKSP 44
#define XT_PITCH 4224
#define WS_EPOCH 0
#define WS_FW 1024
#define WS_F1 4096
#define WS_F2 8192
#define WS_G0 16384
#define WS_W1T 32768
#define WS_W2T (WS_W1T + NFTP * 16 * 1024)
#define WS_HT (WS_W2T + 32 * NKSP * 1024)
#define WS_HACT (WS_HT + DM * XT_PITCH)
#define WS_END (WS_HACT + 64 * NFTP * 1024)

#define STAMP(k, i)
#define STAMP_END(k, i)

__device__ __forceinline__ float bf2f(unsigned v) { return __uint_as_float(v << 16); }
__device__ __forceinline__ float bflo(unsigned v) { return __uint_as_float(v << 16); }
__device__ __forceinline__ float bfhi(unsigned v) { return __uint_as_float(v & 0xffff0000u); }
__device__ __forceinline__ unsigned pk_bf16(float a, float b) {
  f32x2 v = {a, b};
  bf16x2 r = __builtin_convertvector(v, bf16x2);
  return __builtin_bit_cast(unsigned, r);
}
__device__ __forceinline__ unsigned pk_f16(float a, float b) {
  f32x2 v = {a, b};
  f16x2 r = __builtin_convertvector(v, f16x2);
  return __builtin_bit_cast(unsigned, r);
}
__device__ __forceinline__ float rbf(float a) { return bflo(pk_bf16(a, a)); }
__device__ __forceinline__ float h2f_lo(unsigned v) { f16x2 h = __builtin_bit_cast(f16x2, v); return (float)h[0]; }
__device__ __forceinline__ float h2f_hi(unsigned v) { f16x2 h = __builtin_bit_cast(f16x2, v); return (float)h[1]; }
__device__ __forceinline__ unsigned lds_addr(const void* p) { return (unsigned)(size_t)p; }
__device__ __forceinline__ u32x2 tr_read(unsigned ad) {
  s16x4 v = __builtin_amdgcn_ds_read_tr16_b64_v4i16((LDS_AS s16x4*)(size_t)ad);
  return __builtin_bit_cast(u32x2, v);
}
__device__ __forceinline__ void glds16(const void* gsrc, unsigned lds_dst) {
  unsigned keep;
  asm volatile("s_mov_b32 %0, m0\n\ts_mov_b32 m0, %2\n\ts_nop 0\n\tglobal_load_lds_dwordx4 %1, off\n\ts_mov_b32 m0, %0"
               : "=&s"(keep) : "v"(gsrc), "s"(lds_dst) : "memory");
}
__device__ __forceinline__ void glds16_sc1(const void* gsrc, unsigned lds_dst) {
  unsigned keep;
  asm volatile("s_mov_b32 %0, m0\n\ts_mov_b32 m0, %2\n\ts_nop 0\n\tglobal_load_lds_dwordx4 %1, off sc1\n\ts_mov_b32 m0, %0"
               : "=&s"(keep) : "v"(gsrc), "s"(lds_dst) : "memory");
}
__device__ __forceinline__ void store8_dev(void* p, u32x2 v) {
  unsigned long long x = ((unsigned long long)v[1] << 32) | v[0];
  __hip_atomic_store((unsigned long long*)p, x, __ATOMIC_RELAXED, __HIP_MEMORY_SCOPE_AGENT);
}
__device__ __forceinline__ void raw_barrier() {
  asm volatile("s_waitcnt lgkmcnt(0)" ::: "memory");
  __builtin_amdgcn_s_barrier();
  asm volatile("" ::: "memory");
}
template <int CTRL>
__device__ __forceinline__ float dpp_add(float v) {
  int r = __builtin_amdgcn_update_dpp(0, __float_as_int(v), CTRL, 0xf, 0xf, false);
  return v + __int_as_float(r);
}
__device__ __forceinline__ float row16_sum(float v) {
  v = dpp_add<0xB1>(v);
  v = dpp_add<0x4E>(v);
  v = dpp_add<0x141>(v);
  v = dpp_add<0x140>(v);
  return v;
}

struct cpx { float r, i; };
__device__ __forceinline__ cpx cmul(cpx a, cpx b) {
  cpx o; o.r = a.r * b.r - a.i * b.i; o.i = a.r * b.i + a.i * b.r; return o;
}
__device__ __forceinline__ cpx cexp_dir(float lr2, float fr, float e) {
  float mag = __builtin_amdgcn_exp2f(lr2 * e);
  float ph = __builtin_amdgcn_fractf(fr * e);
  cpx o; o.r = mag * __builtin_amdgcn_cosf(ph); o.i = mag * __builtin_amdgcn_sinf(ph); return o;
}

#define K2_ROWB 272
#define K2_COPY (18 * K2_ROWB)
#define K2_UCH (4 * K2_COPY)
#define K2_LDS_U 0
#define K2_LDS_K (39 * 1024)
#define K2_LDS_X (K2_LDS_K + 2 * 4608)
#define KG_KCB 1344
#define KG_LDS_KC (K2_LDS_X + 8192)
#define K2_LDS_Y (KG_LDS_KC + 3072)
#define KG_AROW 528
#define KG_LDS_A K2_LDS_Y
#define KG_LDS_B (KG_LDS_A + 2 * 32 * KG_AROW)
#define P1_LDS_W (KG_LDS_B + 2 * 21 * KG_AROW)
#define P1_LDS_RED (P1_LDS_W + 4 * 5120)
#define K3_LDS_H 0
#define K3_LDS_E 65536
#define K3_LDS_ST (K3_LDS_E + 8 * 9216)
#define K4_LDS_R 0
#define KB_LDS_SIZE (K3_LDS_ST + 1024)
#define SPIN_MAX (1u << 22)

__device__ __forceinline__ f16x8 ld_g(const char* p) {
  u32x2 lo = *(const u32x2*)p, hi = *(const u32x2*)(p + 8);
  u32x4 t = {lo[0], lo[1], hi[0], hi[1]};
  return __builtin_bit_cast(f16x8, t);
}
__device__ __forceinline__ bf16x8 ld_bfrag(unsigned ad) {
  u32x2 l2 = tr_read(ad), h2 = tr_read(ad + 512);
  u32x4 tt = {l2[0], l2[1], h2[0], h2[1]};
  return __builtin_bit_cast(bf16x8, tt);
}

__device__ __forceinline__ bool flags_ok(u32x4 v, unsigned t0, unsigned t1) {
  return v[0] == t0 && v[1] == t1 && v[2] == t0 && v[3] == t1;
}

extern "C" __global__ __launch_bounds__(512, 2) void fused_main(const u16* __restrict__ x,
                                               const u16* __restrict__ lar, const u16* __restrict__ aim,
                                               const u16* __restrict__ cre, const u16* __restrict__ cim,
                                               const u16* __restrict__ ldt, const u16* __restrict__ dsk,
                                               const u16* __restrict__ w1, const u16* __restrict__ b1,
                                               const u16* __restrict__ w2, const u16* __restrict__ b2,
                                               char* __restrict__ ws, u16* __restrict__ out) {
  __shared__ __attribute__((aligned(16))) char smem[KB_LDS_SIZE];
  const int b = blockIdx.x, tid = threadIdx.x, l = tid & 63;
  const int w = __builtin_amdgcn_readfirstlane(tid >> 6);
  const int g = l >> 4, l15 = l & 15;
  const int cp = (b & 7) * 32 + (b >> 3);
  const int tb = (b & 7) * 4 + ((b >> 3) & 3);
  const int cg = b >> 5;
  const int wl = w & 3, wh = w >> 2;
  const unsigned sb = lds_addr(smem);
  const __amdgpu_buffer_rsrc_t rsy = __builtin_amdgcn_make_buffer_rsrc(ws, 0, 32768, 0x00020000);
  float* red = (float*)(smem + P1_LDS_RED);
  volatile LDS_AS unsigned* lnflag = (volatile LDS_AS unsigned*)(size_t)(sb + P1_LDS_RED + 256);
  STAMP(0, 0);
  const unsigned epoch = __hip_atomic_load((const unsigned*)(ws + WS_EPOCH), __ATOMIC_RELAXED, __HIP_MEMORY_SCOPE_AGENT);
  unsigned xr8[8];
  float piv[8];
  u32x4 wv[2][2];
  const int gw = b * 4 + wl;
  if (wh == 0) {
    const unsigned* xr = (const unsigned*)(x + b * 8 * DM + 128 * wl + 2 * l);
#pragma unroll
    for (int r = 0; r < 8; ++r) xr8[r] = xr[r * 256];
#pragma unroll
    for (int r = 0; r < 8; ++r) piv[r] = bf2f(x[(b * 8 + r) * DM]);
    if (l == 0) lnflag[wl] = 0u;
  } else {
#pragma unroll
    for (int it = 0; it < 2; ++it) {
      const int bi = gw + it * 1024;
      if (bi < 688) {
        const int ks = bi / 43, fb = bi - ks * 43;
        const u16* src = w1 + (size_t)(32 * ks + (l >> 2)) * FF + 32 * fb + 8 * (l & 3);
        wv[it][0] = *(const u32x4*)src;
        wv[it][1] = *(const u32x4*)(src + 16 * FF);
      } else if (bi < 1376) {
        const int b2 = bi - 688, ks = b2 >> 4, nbk = b2 & 15;
        const u16* src = w2 + (size_t)(32 * ks + (l >> 2)) * DM + 32 * nbk + 8 * (l & 3);
        wv[it][0] = *(const u32x4*)src;
        wv[it][1] = *(const u32x4*)(src + 16 * DM);
      }
    }
  }
  const int dch = 2 * cp + wh;
  const unsigned v_lar = *(const unsigned*)(lar + dch * NM + 2 * l), v_aim = *(const unsigned*)(aim + dch * NM + 2 * l);
  const unsigned v_cr = *(const unsigned*)(cre + dch * NM + 2 * l), v_ci = *(const unsigned*)(cim + dch * NM + 2 * l);
  const unsigned v_ldt = *(const unsigned*)(ldt + 2 * cp);
  const unsigned dkv = *(const unsigned*)(dsk + 2 * cp);
  asm volatile("" ::: "memory");
  unsigned xc[4];
#pragma unroll
  for (int k = 0; k < 4; ++k) xc[k] = *(const unsigned*)(x + (size_t)(4 * tid + k) * DM + 2 * cp);
  if (l < 35) *(u32x4*)(smem + K2_LDS_U + w * K2_COPY + (l < 34 ? l * 16 : 17 * K2_ROWB + 256)) = (u32x4){0u, 0u, 0u, 0u};
  if (wl == 3 && l < 18) *(u32x4*)(smem + K2_LDS_K + wh * 4608 + (l < 3 ? l : 257 + l) * 16) = (u32x4){0u, 0u, 0u, 0u};
  if (wl == 2 && l < 2) *(u32x4*)(smem + KG_LDS_KC + wh * KG_KCB + l * 16) = (u32x4){0u, 0u, 0u, 0u};
  raw_barrier();
  const unsigned tag0 = epoch + 1u, tag1 = tag0 * 0x9E3779B1u + 0x7F4A7C15u;
  if (wh == 0) {
    float s[16];
#pragma unroll
    for (int r = 0; r < 8; ++r) {
      float a = bflo(xr8[r]) - piv[r], bb = bfhi(xr8[r]) - piv[r];
      s[r] = a + bb;
      s[8 + r] = a * a + bb * bb;
    }
    float t8[8], t4[4];
#pragma unroll
    for (int i = 0; i < 8; ++i) {
      auto sw = __builtin_amdgcn_permlane32_swap(__float_as_uint(s[i]), __float_as_uint(s[8 + i]), false, false);
      t8[i] = __uint_as_float(sw[0]) + __uint_as_float(sw[1]);
    }
#pragma unroll
    for (int i = 0; i < 4; ++i) {
      auto sw = __builtin_amdgcn_permlane16_swap(__float_as_uint(t8[i]), __float_as_uint(t8[4 + i]), false, false);
      t4[i] = row16_sum(__uint_as_float(sw[0]) + __uint_as_float(sw[1]));
    }
    if (l15 == 0) *(f32x4*)(red + wl * 16 + g * 4) = (f32x4){t4[0], t4[1], t4[2], t4[3]};
    asm volatile("s_waitcnt lgkmcnt(0)" ::: "memory");
    if (l == 0) lnflag[wl] = 1u;
    if (wl == 0) {
#pragma unroll 1
      for (unsigned it = 0; it < (1u << 20); ++it) {
        unsigned f = lnflag[l & 3];
        if (__all(f == 1u)) break;
        __builtin_amdgcn_s_sleep(1);
      }
      asm volatile("" ::: "memory");
      const int r = l & 7;
      float t1 = red[r] + red[16 + r] + red[32 + r] + red[48 + r];
      float t2 = red[8 + r] + red[24 + r] + red[40 + r] + red[56 + r];
      float pv = piv[0];
#pragma unroll
      for (int k = 1; k < 8; ++k) pv = r == k ? piv[k] : pv;
      float dm = t1 * (1.f / 512.f);
      float var = fmaxf(t2 * (1.f / 512.f) - dm * dm, 0.f);
      const unsigned md = pk_bf16(pv + dm, __builtin_amdgcn_sqrtf(rbf(rbf(var) + 1e-5f)));
      if (l < 8) store8_dev(ws + WS_G0 + (b * 8 + l) * 8, (u32x2){md, tag0});
    }
  } else {
    char* wlp = smem + P1_LDS_W + wl * 5120;
    const __amdgpu_buffer_rsrc_t rwt = __builtin_amdgcn_make_buffer_rsrc(ws + WS_W1T, 0, (NFTP * 16 + 32 * NKSP) * 1024, 0x00020000);
    if (wl == 2 && b < 32) __builtin_amdgcn_raw_buffer_store_b128((u32x4){0u, 0u, 0u, 0u}, rwt, (86 * 16 + b) * 1024 + l * 16, 0, 16);
#pragma unroll
    for (int it = 0; it < 2; ++it) {
      const int bi = gw + it * 1024;
      if (bi < 1376) {
        char* t = wlp + it * 2560;
        *(u32x4*)(t + (l >> 2) * 80 + (l & 3) * 16) = wv[it][0];
        *(u32x4*)(t + (16 + (l >> 2)) * 80 + (l & 3) * 16) = wv[it][1];
        const bool isw1 = bi < 688;
        const int q = l15 >> 2, p = l15 & 3;
        const unsigned a0 = lds_addr(t) + (isw1 ? (8 * g + q) : (4 * g + q)) * 80 + p * 8;
        const unsigned dr = isw1 ? 4 * 80 : 16 * 80;
#pragma unroll
        for (int ct = 0; ct < 2; ++ct) {
          u32x2 lo = tr_read(a0 + ct * 32), hi = tr_read(a0 + ct * 32 + dr);
          u32x4 o = {lo[0], lo[1], hi[0], hi[1]};
          unsigned off;
          if (isw1) { const int ks = bi / 43, fb = bi - ks * 43; off = ((2 * fb + ct) * 16 + ks) * 1024; }
          else { const int b2 = bi - 688, ks = b2 >> 4; off = (NFTP * 16 + (2 * (b2 & 15) + ct) * NKSP + ks) * 1024; }
          __builtin_amdgcn_raw_buffer_store_b128(o, rwt, off + l * 16, 0, 16);
        }
      }
    }
  }
  STAMP(0, 1);
  float ksc;
  {
    const int part = wh ? (wl ^ 2) : wl;
    const float dt = rbf(__expf(wh ? bfhi(v_ldt) : bflo(v_ldt)));
    float lr2[2], fr[2];
    lr2[0] = (-rbf(__expf(bflo(v_lar))) * dt) * 1.4426950408889634f;
    lr2[1] = (-rbf(__expf(bfhi(v_lar))) * dt) * 1.4426950408889634f;
    fr[0] = (bflo(v_aim) * dt) * 0.15915494309189535f;
    fr[1] = (bfhi(v_aim) * dt) * 0.15915494309189535f;
    float cm = fmaxf(fmaxf(fabsf(bflo(v_cr)), fabsf(bfhi(v_cr))), fmaxf(fabsf(bflo(v_ci)), fabsf(bfhi(v_ci))));
#pragma unroll
    for (int o = 32; o > 0; o >>= 1) cm = fmaxf(cm, __shfl_xor(cm, o));
    ksc = 2.f * dt * cm * 128.f;
    if (part < 2) {
      const float cinv = cm > 0.f ? 1.f / cm : 0.f;
      char* Aimg = smem + KG_LDS_A + wh * 32 * KG_AROW + (16 * part) * KG_AROW + l * 8;
      cpx P[2][2], z8[2];
      const float e0 = 128.f * part;
#pragma unroll
      for (int m = 0; m < 2; ++m) {
        cpx C; C.r = (m ? bfhi(v_cr) : bflo(v_cr)) * cinv; C.i = (m ? bfhi(v_ci) : bflo(v_ci)) * cinv;
        z8[m] = cexp_dir(lr2[m], fr[m], 8.f);
        P[m][0] = cmul(C, cexp_dir(lr2[m], fr[m], e0));
        P[m][1] = cmul(C, cexp_dir(lr2[m], fr[m], e0 + 64.f));
      }
#pragma unroll
      for (int a = 0; a < 8; ++a) {
#pragma unroll
        for (int k = 0; k < 2; ++k) {
          u32x2 o = {pk_f16(P[0][k].r, P[0][k].i), pk_f16(P[1][k].r, P[1][k].i)};
          *(u32x2*)(Aimg + (8 * k + a) * KG_AROW) = o;
          if (a < 7) { P[0][k] = cmul(P[0][k], z8[0]); P[1][k] = cmul(P[1][k], z8[1]); }
        }
      }
    } else {
      char* Bimg = smem + KG_LDS_B + wh * 21 * KG_AROW + l * 8;
#define PUTB(col, v0, v1) { cpx _a = (v0), _b = (v1); u32x2 _o = {pk_f16(_a.r, -_a.i), pk_f16(_b.r, -_b.i)}; *(u32x2*)(Bimg + (col) * KG_AROW) = _o; }
      if (part == 2) {
        cpx z[2][8], e256[2];
#pragma unroll
        for (int m = 0; m < 2; ++m) {
          z[m][0].r = 1.f; z[m][0].i = 0.f;
          z[m][1] = cexp_dir(lr2[m], fr[m], 1.f);
          z[m][2] = cmul(z[m][1], z[m][1]); z[m][3] = cmul(z[m][2], z[m][1]); z[m][4] = cmul(z[m][2], z[m][2]);
          z[m][5] = cmul(z[m][4], z[m][1]); z[m][6] = cmul(z[m][4], z[m][2]); z[m][7] = cmul(z[m][4], z[m][3]);
          e256[m] = cexp_dir(lr2[m], fr[m], 256.f);
        }
#pragma unroll
        for (int k = 0; k < 8; ++k) PUTB(k, z[0][k], z[1][k]);
        PUTB(8, e256[0], e256[1]);
#pragma unroll
        for (int k = 1; k < 4; ++k) PUTB(8 + k, cmul(e256[0], z[0][2 * k]), cmul(e256[1], z[1][2 * k]));
      } else {
        cpx z4[2], e256[2], e512[2], e768[2], e1024[2];
#pragma unroll
        for (int m = 0; m < 2; ++m) {
          z4[m] = cexp_dir(lr2[m], fr[m], 4.f);
          e256[m] = cexp_dir(lr2[m], fr[m], 256.f);
          e1024[m] = cexp_dir(lr2[m], fr[m], 1024.f);
          e512[m] = cmul(e256[m], e256[m]); e768[m] = cmul(e512[m], e256[m]);
        }
        PUTB(12, e512[0], e512[1]); PUTB(13, cmul(e512[0], z4[0]), cmul(e512[1], z4[1]));
        PUTB(14, e768[0], e768[1]); PUTB(15, cmul(e768[0], z4[0]), cmul(e768[1], z4[1]));
        PUTB(16, e1024[0], e1024[1]);
        PUTB(17, cmul(e1024[0], e256[0]), cmul(e1024[1], e256[1]));
        PUTB(18, cmul(e1024[0], e512[0]), cmul(e1024[1], e512[1]));
        PUTB(19, cmul(e1024[0], e768[0]), cmul(e1024[1], e768[1]));
        PUTB(20, cmul(e1024[0], e1024[0]), cmul(e1024[1], e1024[1]));
      }
#undef PUTB
    }
    STAMP(0, 2);
    asm volatile("s_waitcnt vmcnt(0)" ::: "memory");
    raw_barrier();
    if (tid == 448) store8_dev(ws + WS_FW + b * 8, (u32x2){tag0, tag1});
    {
      const int rt = wl >> 1, ct = wl & 1;
      const char* Ab = smem + KG_LDS_A + wh * 32 * KG_AROW + (16 * rt + l15) * KG_AROW + g * 16;
      const int bc = 16 * ct + l15;
      const char* Bb = smem + KG_LDS_B + wh * 21 * KG_AROW + (bc < 21 ? bc : 20) * KG_AROW + g * 16;
      f16x8 af[8], bfr[8];
#pragma unroll
      for (int q = 0; q < 8; ++q) { af[q] = *(const f16x8*)(Ab + q * 64); bfr[q] = *(const f16x8*)(Bb + q * 64); }
      f32x4 acc = {0, 0, 0, 0};
#pragma unroll
      for (int q = 0; q < 8; ++q) acc = __builtin_amdgcn_mfma_f32_16x16x32_f16(af[q], bfr[q], acc, 0, 0, 0);
      _Float16* kc = (_Float16*)(smem + KG_LDS_KC + wh * KG_KCB + 32);
      const int a0 = 16 * rt + 4 * g;
      int base, stride;
      if (ct == 0) {
        if (l15 < 8) { base = l15; stride = 8; }
        else if (l15 < 12) { base = 256 + (l15 - 8); stride = 4; }
        else { base = 384 + 64 * ((l15 - 12) >> 1) + (l15 & 1); stride = 2; }
      } else {
        base = 512 + 32 * l15; stride = 1;
      }
#pragma unroll
      for (int r = 0; r < 4; ++r) {
        const bool ok = ct == 0 || l15 < 4 || (l15 == 4 && a0 + r == 0);
        if (ok) kc[base + stride * (a0 + r)] = (_Float16)(acc[r] * (1.f / 128.f));
      }
    }
  }
  STAMP(0, 3);
  const int p0 = 4 * tid;
  const unsigned upos = ((p0 >> 7) + 2) * K2_ROWB + (p0 & 127) * 2;
  const bool dup = (p0 & 127) < 8;
  u32x2 uo[2];
  {
    u32x4 ga, gb;
#pragma unroll 1
    for (unsigned it = 0; it < SPIN_MAX; ++it) {
      ga = __builtin_amdgcn_raw_buffer_load_b128(rsy, WS_G0 + tid * 32, 0, 16);
      gb = __builtin_amdgcn_raw_buffer_load_b128(rsy, WS_G0 + tid * 32 + 16, 0, 16);
      if (__all(ga[1] == tag0 && ga[3] == tag0 && gb[1] == tag0 && gb[3] == tag0)) break;
      __builtin_amdgcn_s_sleep(1);
    }
    const unsigned md[4] = {ga[0], ga[2], gb[0], gb[2]};
    unsigned ub[4];
#pragma unroll
    for (int k = 0; k < 4; ++k) {
      const float mb = bflo(md[k]), rd = __builtin_amdgcn_rcpf(bfhi(md[k]));
      unsigned dd = pk_bf16(bflo(xc[k]) - mb, bfhi(xc[k]) - mb);
      unsigned uu = pk_bf16(bflo(dd) * rd, bfhi(dd) * rd);
      ub[k] = pk_f16(bflo(uu), bfhi(uu));
    }
    uo[0] = (u32x2){__builtin_amdgcn_perm(ub[1], ub[0], 0x05040100u), __builtin_amdgcn_perm(ub[3], ub[2], 0x05040100u)};
    uo[1] = (u32x2){__builtin_amdgcn_perm(ub[1], ub[0], 0x07060302u), __builtin_amdgcn_perm(ub[3], ub[2], 0x07060302u)};
    const u32x2 x0 = {__builtin_amdgcn_perm(xc[1], xc[0], 0x05040100u), __builtin_amdgcn_perm(xc[3], xc[2], 0x05040100u)};
    const u32x2 x1 = {__builtin_amdgcn_perm(xc[1], xc[0], 0x07060302u), __builtin_amdgcn_perm(xc[3], xc[2], 0x07060302u)};
    *(u32x2*)(smem + K2_LDS_X + p0 * 2) = x0;
    *(u32x2*)(smem + K2_LDS_X + 4096 + p0 * 2) = x1;
#pragma unroll
    for (int c = 0; c < 2; ++c) {
      *(u32x2*)(smem + K2_LDS_U + c * K2_UCH + upos) = uo[c];
      if (dup) *(u32x2*)(smem + K2_LDS_U + c * K2_UCH + upos - K2_ROWB + 256) = uo[c];
    }
  }
  STAMP(0, 4);
  raw_barrier();
#pragma unroll
  for (int c = 0; c < 2; ++c) {
    const u32x2 nx = *(const u32x2*)(smem + K2_LDS_U + c * K2_UCH + upos + 8);
    const unsigned d0 = uo[c][0], d1 = uo[c][1], d2 = nx[0], d3 = nx[1];
    u32x2 cs3[3];
    cs3[0] = (u32x2){__builtin_amdgcn_alignbit(d1, d0, 16), __builtin_amdgcn_alignbit(d2, d1, 16)};
    cs3[1] = (u32x2){d1, d2};
    cs3[2] = (u32x2){__builtin_amdgcn_alignbit(d2, d1, 16), __builtin_amdgcn_alignbit(d3, d2, 16)};
#pragma unroll
    for (int sft = 1; sft < 4; ++sft) {
      char* dst = smem + K2_LDS_U + c * K2_UCH + sft * K2_COPY + upos;
      *(u32x2*)dst = cs3[sft - 1];
      if (dup) *(u32x2*)(dst - K2_ROWB + 256) = cs3[sft - 1];
    }
    if (tid == 0) {
      char* dst = smem + K2_LDS_U + c * K2_UCH + K2_ROWB + 124 * 2;
      *(u32x2*)(dst + 1 * K2_COPY) = (u32x2){0u, d0 << 16};
      *(u32x2*)(dst + 2 * K2_COPY) = (u32x2){0u, d0};
      *(u32x2*)(dst + 3 * K2_COPY) = (u32x2){d0 << 16, __builtin_amdgcn_alignbit(d1, d0, 16)};
    }
  }
  {
    const int c2 = (tid & 255) + 1;
    int ih; unsigned tbl;
    if (c2 <= 32) { ih = 8 * c2; tbl = 0x76543210u; }
    else if (c2 <= 64) { ih = 256 + 4 * (c2 - 32); tbl = 0x43222100u; }
    else if (c2 <= 128) { ih = 384 + 2 * (c2 - 64); tbl = 0x22111000u; }
    else { ih = 512 + (c2 - 128); tbl = 0x11100000u | ((unsigned)(c2 & 1) << 16); }
    const u16* kc0 = (const u16*)(smem + KG_LDS_KC + wh * KG_KCB + 32);
    const u16* kc = kc0 + ih;
    unsigned v[8];
#pragma unroll
    for (int j = 0; j < 8; ++j) v[j] = kc[-(int)((tbl >> (4 * j)) & 15u)];
    if (c2 == 256) v[0] = 0u;
    u32x4 o = {v[0] | (v[1] << 16), v[2] | (v[3] << 16), v[4] | (v[5] << 16), v[6] | (v[7] << 16)};
    *(u32x4*)(smem + K2_LDS_K + wh * 4608 + (259 - c2) * 16) = o;
    if (c2 == 1) *(u32x4*)(smem + K2_LDS_K + wh * 4608 + 259 * 16) = (u32x4){(unsigned)kc0[0], 0u, 0u, 0u};
  }
  if (w == 3) {
#pragma unroll 1
    for (unsigned it = 0; it < SPIN_MAX; ++it) {
      u32x4 fa = __builtin_amdgcn_raw_buffer_load_b128(rsy, WS_FW + l * 32, 0, 16);
      u32x4 fb = __builtin_amdgcn_raw_buffer_load_b128(rsy, WS_FW + l * 32 + 16, 0, 16);
      if (__all(flags_ok(fa, tag0, tag1) && flags_ok(fb, tag0, tag1))) break;
      __builtin_amdgcn_s_sleep(1);
    }
  }
  STAMP(0, 5);
  raw_barrier();
  const int myft = cg * 11 + 3 * wl;
  bf16x8 wf[3][8];
  const __amdgpu_buffer_rsrc_t rw1 = __builtin_amdgcn_make_buffer_rsrc(ws + WS_W1T, 0, NFTP * 16 * 1024, 0x00020000);
  const unsigned w1o = (myft * 16 + wh * 8) * 1024;
#define LDW1(a, k) __builtin_bit_cast(bf16x8, __builtin_amdgcn_raw_buffer_load_b128(rw1, l * 16, w1o + (a) * 16 * 1024 + (k) * 1024, 16))
  STAMP(1, 1);
  const u32x2 b2v = *(const u32x2*)(b2 + cg * 64 + (w >> 1) * 16 + 4 * g);
  u32x2 b1v[3];
#pragma unroll
  for (int a = 0; a < 3; ++a) {
    const int f = 16 * (myft + a) + 4 * g;
    b1v[a] = *(const u32x2*)(b1 + (f < FF ? f : 0));
  }
  {
    f32x4 acc[8];
#pragma unroll
    for (int i = 0; i < 8; ++i) acc[i] = (f32x4){0, 0, 0, 0};
    const int phi = l15 & 7, nu = l15 >> 3;
    const char* Fb = smem + K2_LDS_K + wh * 4608 + (259 - l15 + g) * 16 + 192 - 64 * wl;
    const char* Gb = smem + K2_LDS_U + wh * K2_UCH + (phi & 3) * K2_COPY + (nu + 2) * K2_ROWB + ((phi & 4) + 8 * g) * 2 + 192 - 64 * wl;
#pragma unroll
    for (int rr = 0; rr < 2; ++rr) {
      f16x8 F[8], G[8];
#pragma unroll
      for (int q = 0; q < 8; ++q) { G[q] = ld_g(Gb - rr * K2_ROWB + q * 2 * K2_ROWB); F[q] = *(const f16x8*)(Fb - rr * 256 - q * 512); }
      __builtin_amdgcn_sched_barrier(0);
#pragma unroll
      for (int k = 0; k < 8; ++k) wf[rr][k] = LDW1(rr, k);
#pragma unroll
      for (int e = 0; e < 8; ++e)
#pragma unroll
        for (int q = 0; q + e < 8; ++q)
          acc[q + e] = __builtin_amdgcn_mfma_f32_16x16x32_f16(G[e], F[q], acc[q + e], 0, 0, 0);
#pragma unroll
      for (int k = 0; k < 8; ++k) {
        __builtin_amdgcn_sched_group_barrier(0x008, 4, 0);
        __builtin_amdgcn_sched_group_barrier(0x020, 1, 0);
      }
      __builtin_amdgcn_sched_barrier(0);
      if (rr == 0 && wl == 0) {
        f16x8 Gm = ld_g(Gb - 2 * K2_ROWB), F8 = *(const f16x8*)(Fb - 8 * 512);
#pragma unroll
        for (int i = 0; i < 7; ++i) acc[i] = __builtin_amdgcn_mfma_f32_16x16x32_f16(Gm, F[i + 1], acc[i], 0, 0, 0);
        acc[7] = __builtin_amdgcn_mfma_f32_16x16x32_f16(Gm, F8, acc[7], 0, 0, 0);
      }
    }
    STAMP(1, 2);
#pragma unroll
    for (int i = 0; i < 8; ++i) *(f32x4*)(smem + K2_LDS_Y + (w * 8 + i) * 1024 + l * 16) = acc[i];
    raw_barrier();
    const float dk = wh ? bfhi(dkv) : bflo(dkv);
    const int tl = 128 * (g >> 1) + 8 * l15 + 4 * (g & 1);
#pragma unroll
    for (int i = 0; i < 2; ++i) {
      const int ti = 2 * wl + i;
      f32x4 o = *(const f32x4*)(smem + K2_LDS_Y + ((wh * 4 + 0) * 8 + ti) * 1024 + l * 16);
#pragma unroll
      for (int k = 1; k < 4; ++k) o += *(const f32x4*)(smem + K2_LDS_Y + ((wh * 4 + k) * 8 + ti) * 1024 + l * 16);
      const int t0 = 256 * ti + tl;
      u32x2 uu = *(const u32x2*)(smem + K2_LDS_U + wh * K2_UCH + ((t0 >> 7) + 2) * K2_ROWB + (t0 & 127) * 2);
      u32x2 xx = *(const u32x2*)(smem + K2_LDS_X + wh * 4096 + t0 * 2);
      u32x2 res;
#pragma unroll
      for (int j = 0; j < 2; ++j) {
        unsigned yy = pk_bf16(o[2 * j] * ksc, o[2 * j + 1] * ksc);
        unsigned ud = pk_bf16(h2f_lo(uu[j]) * dk, h2f_hi(uu[j]) * dk);
        unsigned z = pk_bf16(bflo(yy) + bflo(ud), bfhi(yy) + bfhi(ud));
        res[j] = pk_bf16(bflo(xx[j]) + bflo(z), bfhi(xx[j]) + bfhi(z));
      }
      store8_dev(ws + WS_HT + (size_t)(2 * cp + wh) * XT_PITCH + t0 * 2, res);
    }
  }
  STAMP(1, 3);
  asm volatile("s_waitcnt vmcnt(0)" ::: "memory");
  raw_barrier();
  if (tid == 192) store8_dev(ws + WS_F1 + b * 8, (u32x2){tag0, tag1});
  STAMP(1, 4);
  const int ks0 = w * 5 + (w < 3 ? w : 3);
  bf16x8 w2f[6][4];
  const __amdgpu_buffer_rsrc_t rw2 = __builtin_amdgcn_make_buffer_rsrc(ws + WS_W2T, 0, 32 * NKSP * 1024, 0x00020000);
  const unsigned w2o = ((cg * 4) * NKSP + ks0) * 1024;
#define LDW2(k, a) __builtin_bit_cast(bf16x8, __builtin_amdgcn_raw_buffer_load_b128(rw2, l * 16, w2o + ((a) * NKSP + (k)) * 1024, 16))
  if (wl < 3) {
#pragma unroll
    for (int k = 0; k < 8; ++k) wf[2][k] = LDW1(2, k);
  } else {
#pragma unroll
    for (int k = 0; k < 8; ++k) wf[2][k] = wf[1][k];
  }
  __builtin_amdgcn_sched_barrier(0);
  const u32x4 onesu = {0x3f803f80u, 0x3f803f80u, 0x3f803f80u, 0x3f803f80u};
  const bf16x8 onesf = __builtin_bit_cast(bf16x8, onesu);
  {
    f32x4 cq[2];
#pragma unroll
    for (int a = 0; a < 2; ++a) cq[a] = (f32x4){0, 0, 0, 0};
#pragma unroll
    for (int k = 0; k < 8; ++k)
#pragma unroll
      for (int a = 0; a < 2; ++a) cq[a] = __builtin_amdgcn_mfma_f32_16x16x32_bf16(wf[a][k], onesf, cq[a], 0, 0, 0);
#pragma unroll
    for (int a = 0; a < 2; ++a) *(f32x4*)(smem + K3_LDS_E + (w * 9 + 6 + a) * 1024 + l * 16) = cq[a];
  }
  STAMP(1, 5);
  if (w == 3) {
#pragma unroll 1
    for (unsigned it = 0; it < SPIN_MAX; ++it) {
      u32x4 fa = __builtin_amdgcn_raw_buffer_load_b128(rsy, WS_F1 + l * 32, 0, 16);
      u32x4 fb = __builtin_amdgcn_raw_buffer_load_b128(rsy, WS_F1 + l * 32 + 16, 0, 16);
      if (__all(flags_ok(fa, tag0, tag1) && flags_ok(fb, tag0, tag1))) break;
      __builtin_amdgcn_s_sleep(1);
    }
    if (b == 0 && l == 0) __hip_atomic_store((unsigned*)(ws + WS_EPOCH), tag0, __ATOMIC_RELAXED, __HIP_MEMORY_SCOPE_AGENT);
  }
  raw_barrier();
  STAMP(2, 0);
  asm volatile("" : "+v"(wf[2][0]), "+v"(wf[2][1]), "+v"(wf[2][2]), "+v"(wf[2][3]), "+v"(wf[2][4]), "+v"(wf[2][5]), "+v"(wf[2][6]), "+v"(wf[2][7]) : "v"(b2v), "v"(b1v[0]), "v"(b1v[1]), "v"(b1v[2]) : "memory");
#pragma unroll
  for (int it = 0; it < 8; ++it) {
    const int j = it * 8 + w;
    const int row = j * 8 + (l >> 3), pc = l & 7;
    const int swz = ((row >> 1) & 1) | (((row >> 3) & 1) << 1);
    const int lc = (((pc >> 1) ^ swz) << 1) | (pc & 1);
    glds16_sc1(ws + WS_HT + (size_t)row * XT_PITCH + tb * 128 + lc * 16, sb + K3_LDS_H + j * 1024);
  }
  {
    f32x4 cq = {0, 0, 0, 0};
#pragma unroll
    for (int k = 0; k < 8; ++k) cq = __builtin_amdgcn_mfma_f32_16x16x32_bf16(wf[2][k], onesf, cq, 0, 0, 0);
    *(f32x4*)(smem + K3_LDS_E + (w * 9 + 8) * 1024 + l * 16) = cq;
  }
  asm volatile("s_waitcnt vmcnt(0)" ::: "memory");
  raw_barrier();
  STAMP(2, 1);
  const int q4 = l15 >> 2, p4 = l15 & 3;
  const __amdgpu_buffer_rsrc_t rs = __builtin_amdgcn_make_buffer_rsrc(ws + WS_HACT, 0, 64 * NFTP * 1024, 0x00020000);
  u32x2 hres[2];
  {
    const int swz = ((q4 >> 1) & 1) | ((g & 1) << 1);
    const unsigned lbase = sb + K3_LDS_H + wh * 8 * 4096 + (8 * g + q4) * 128 + p4 * 8;
    unsigned ad[4];
#pragma unroll
    for (int t = 0; t < 4; ++t) ad[t] = lbase + (((t ^ (2 * wh)) ^ swz) << 5);
    const unsigned sa = lbase + ((wl ^ swz) << 5);
    const unsigned vo2 = wl < 3 ? (unsigned)(l * 16) : 0x7f000000u;
    f32x4 acc[3][2];
    bf16x8 bf[2][2];
    auto finish = [&](int p) {
      f32x4 cs[3], bb[3];
      f32x2 st[2];
#pragma unroll
      for (int a = 0; a < 3; ++a) {
        bb[a] = (f32x4){bflo(b1v[a][0]), bfhi(b1v[a][0]), bflo(b1v[a][1]), bfhi(b1v[a][1])};
        cs[a] = *(const f32x4*)(smem + K3_LDS_E + (w * 9 + 6 + a) * 1024 + l * 16) + *(const f32x4*)(smem + K3_LDS_E + ((w ^ 4) * 9 + 6 + a) * 1024 + l * 16);
      }
#pragma unroll
      for (int t = 0; t < 2; ++t) {
        const int tr = (2 * p + t) * 16 + l15;
        f32x2 p0 = *(const f32x2*)(smem + K3_LDS_ST + tr * 8), p1 = *(const f32x2*)(smem + K3_LDS_ST + (64 + tr) * 8);
        float mean = (p0[0] + p1[0]) * (1.f / 512.f);
        float var = fmaxf((p0[1] + p1[1]) * (1.f / 512.f) - mean * mean, 0.f);
        float m1 = rbf(mean);
        float r1 = __builtin_amdgcn_rcpf(rbf(__builtin_amdgcn_sqrtf(rbf(rbf(var) + 1e-5f))));
        float mean2 = (mean - m1) * r1, var2 = var * r1 * r1;
        float m2 = rbf(mean2);
        float r2 = __builtin_amdgcn_rcpf(rbf(__builtin_amdgcn_sqrtf(rbf(rbf(var2) + 1e-5f))));
        st[t] = (f32x2){r1 * r2, -(m1 * r1 + m2) * r2};
      }
#pragma unroll
      for (int a = 0; a < 3; ++a)
#pragma unroll
        for (int t = 0; t < 2; ++t) acc[a][t] += *(const f32x4*)(smem + K3_LDS_E + ((w ^ 4) * 9 + a * 2 + t) * 1024 + l * 16);
      const unsigned so = ((tb * 2 + p) * NFTP + myft) * 1024;
      const f32x2 c1 = {-0.10294324f, -0.10294324f}, c0 = {-2.30220819f, -2.30220819f}, one = {1.f, 1.f};
      f32x2 v[12], q[12];
#pragma unroll
      for (int i = 0; i < 12; ++i) {
        const int a = i >> 2, t = (i >> 1) & 1, r = 2 * (i & 1);
        const f32x2 a2 = {st[t][0], st[t][0]}, b2 = {st[t][1], st[t][1]};
        v[i] = __builtin_elementwise_fma(a2, (f32x2){acc[a][t][r], acc[a][t][r + 1]},
                                         __builtin_elementwise_fma(b2, (f32x2){cs[a][r], cs[a][r + 1]}, (f32x2){bb[a][r], bb[a][r + 1]}));
      }
      __builtin_amdgcn_sched_barrier(0);
#pragma unroll
      for (int i = 0; i < 12; ++i) q[i] = v[i] * v[i];
      __builtin_amdgcn_sched_barrier(0);
#pragma unroll
      for (int i = 0; i < 12; ++i) q[i] = __builtin_elementwise_fma(q[i], c1, c0);
      __builtin_amdgcn_sched_barrier(0);
#pragma unroll
      for (int i = 0; i < 12; ++i) q[i] = q[i] * v[i];
      __builtin_amdgcn_sched_barrier(0);
#pragma unroll
      for (int i = 0; i < 12; ++i) q[i] = (f32x2){__builtin_amdgcn_exp2f(q[i][0]), __builtin_amdgcn_exp2f(q[i][1])};
      __builtin_amdgcn_sched_barrier(0);
#pragma unroll
      for (int i = 0; i < 12; ++i) q[i] = q[i] + one;
      __builtin_amdgcn_sched_barrier(0);
#pragma unroll
      for (int i = 0; i < 12; ++i) q[i] = (f32x2){__builtin_amdgcn_rcpf(q[i][0]), __builtin_amdgcn_rcpf(q[i][1])};
      __builtin_amdgcn_sched_barrier(0);
#pragma unroll
      for (int i = 0; i < 12; ++i) v[i] = v[i] * q[i];
      __builtin_amdgcn_sched_barrier(0);
#pragma unroll
      for (int a = 0; a < 3; ++a) {
        u32x4 o = {pk_bf16(v[4 * a][0], v[4 * a][1]), pk_bf16(v[4 * a + 1][0], v[4 * a + 1][1]),
                   pk_bf16(v[4 * a + 2][0], v[4 * a + 2][1]), pk_bf16(v[4 * a + 3][0], v[4 * a + 3][1])};
        __builtin_amdgcn_raw_buffer_store_b128(o, rs, (a < 2 ? (unsigned)(l * 16) : vo2) + so + a * 1024, 0, 16);
      }
      __builtin_amdgcn_sched_barrier(0);
    };
    {
      f32x4 sS = {0, 0, 0, 0}, sQ = {0, 0, 0, 0};
      f32x4 acs[3][2];
      bf16x8 bg[2][2], sf[2];
#pragma unroll
      for (int a = 0; a < 3; ++a)
#pragma unroll
        for (int t = 0; t < 2; ++t) { acc[a][t] = (f32x4){0, 0, 0, 0}; acs[a][t] = (f32x4){0, 0, 0, 0}; }
      bf[0][0] = ld_bfrag(ad[0]); bf[0][1] = ld_bfrag(ad[1]); bg[0][0] = ld_bfrag(ad[2]); bg[0][1] = ld_bfrag(ad[3]); sf[0] = ld_bfrag(sa);
      __builtin_amdgcn_sched_barrier(0);
#pragma unroll
      for (int ks = 0; ks < 8; ++ks) {
        const int cur = ks & 1, nx = cur ^ 1;
        if (ks < 7) {
          bf[nx][0] = ld_bfrag(ad[0] + (ks + 1) * 4096); bf[nx][1] = ld_bfrag(ad[1] + (ks + 1) * 4096);
          bg[nx][0] = ld_bfrag(ad[2] + (ks + 1) * 4096); bg[nx][1] = ld_bfrag(ad[3] + (ks + 1) * 4096);
          sf[nx] = ld_bfrag(sa + (ks + 1) * 4096);
        }
        w2f[ks >> 1][(ks & 1) * 2] = LDW2(ks >> 1, (ks & 1) * 2);
        w2f[ks >> 1][(ks & 1) * 2 + 1] = LDW2(ks >> 1, (ks & 1) * 2 + 1);
#pragma unroll
        for (int t = 0; t < 2; ++t)
#pragma unroll
          for (int a = 0; a < 3; ++a) acs[a][t] = __builtin_amdgcn_mfma_f32_16x16x32_bf16(wf[a][ks], bg[cur][t], acs[a][t], 0, 0, 0);
#pragma unroll
        for (int t = 0; t < 2; ++t)
#pragma unroll
          for (int a = 0; a < 3; ++a) acc[a][t] = __builtin_amdgcn_mfma_f32_16x16x32_bf16(wf[a][ks], bf[cur][t], acc[a][t], 0, 0, 0);
        sS = __builtin_amdgcn_mfma_f32_16x16x32_bf16(onesf, sf[cur], sS, 0, 0, 0);
        sQ = __builtin_amdgcn_mfma_f32_16x16x32_bf16(sf[cur], sf[cur], sQ, 0, 0, 0);
#pragma unroll
        for (int k = 0; k < 10; ++k) {
          __builtin_amdgcn_sched_group_barrier(0x008, 1, 0);
          if (ks < 7) __builtin_amdgcn_sched_group_barrier(0x100, 1, 0);
          if (k == 0 || k == 6) __builtin_amdgcn_sched_group_barrier(0x020, 1, 0);
        }
        __builtin_amdgcn_sched_barrier(0);
      }
      STAMP(2, 2);
#pragma unroll
      for (int a = 0; a < 3; ++a)
#pragma unroll
        for (int t = 0; t < 2; ++t) *(f32x4*)(smem + K3_LDS_E + (w * 9 + a * 2 + t) * 1024 + l * 16) = acs[a][t];
      float dq = (l15 & 2) ? ((l15 & 1) ? sQ[3] : sQ[2]) : ((l15 & 1) ? sQ[1] : sQ[0]);
      float s2 = __shfl(dq, l15 + 16 * (l15 >> 2));
      if (l < 16) *(f32x2*)(smem + K3_LDS_ST + (wh * 64 + wl * 16 + l) * 8) = (f32x2){sS[0], s2};
    }
#pragma unroll
    for (int a = 0; a < 4; ++a) w2f[4][a] = LDW2(4, a);
    if (w < 3) {
#pragma unroll
      for (int a = 0; a < 4; ++a) w2f[5][a] = LDW2(5, a);
    }
    {
      const int row = cg * 64 + (w >> 1) * 16 + 4 * g + q4;
      const int swz2 = ((row >> 1) & 1) | (((row >> 3) & 1) << 1);
      const unsigned hb = sb + K3_LDS_H + row * 128 + p4 * 8;
#pragma unroll
      for (int i = 0; i < 2; ++i) hres[i] = tr_read(hb + (((2 * i + (w & 1)) ^ swz2) << 5));
    }
    raw_barrier();
    STAMP(2, 3);
    finish(wh);
  }
  STAMP(2, 4);
  asm volatile("s_waitcnt vmcnt(0)" ::: "memory");
  raw_barrier();
  STAMP(2, 5);
  if (w == 0) {
    if (l == 0) store8_dev(ws + WS_F2 + tb * 128 + cg * 8, (u32x2){tag0, tag1});
#pragma unroll 1
    for (unsigned it = 0; it < SPIN_MAX; ++it) {
      u32x4 fa = __builtin_amdgcn_raw_buffer_load_b128(rsy, WS_F2 + tb * 128 + (l & 3) * 16, 0, 16);
      if (__all(flags_ok(fa, tag0, tag1))) break;
      __builtin_amdgcn_s_sleep(1);
    }
  }
  raw_barrier();
  STAMP(3, 0);
  {
    const unsigned hvo = ((tb * 2) * NFTP + ks0 * 2) * 1024;
    u32x4 hv[2][6][2];
#pragma unroll
    for (int p = 0; p < 2; ++p) {
#pragma unroll
      for (int k = 0; k < 5; ++k)
#pragma unroll
        for (int f = 0; f < 2; ++f)
          hv[p][k][f] = __builtin_amdgcn_raw_buffer_load_b128(rs, l * 16, hvo + (p * NFTP + 2 * k + f) * 1024, 16);
    }
    if (w < 3) {
#pragma unroll
      for (int p = 0; p < 2; ++p)
#pragma unroll
        for (int f = 0; f < 2; ++f)
          hv[p][5][f] = __builtin_amdgcn_raw_buffer_load_b128(rs, l * 16, hvo + (p * NFTP + 10 + f) * 1024, 16);
    }
    __builtin_amdgcn_sched_barrier(0);
#pragma unroll
    for (int p = 0; p < 2; ++p) {
      f32x4 acc[4][2];
#pragma unroll
      for (int a = 0; a < 4; ++a) { acc[a][0] = (f32x4){0, 0, 0, 0}; acc[a][1] = (f32x4){0, 0, 0, 0}; }
#pragma unroll
      for (int k = 0; k < 6; ++k) {
        if (k < 5 || w < 3) {
          u32x4 x0 = {hv[p][k][0][0], hv[p][k][0][1], hv[p][k][1][0], hv[p][k][1][1]};
          u32x4 x1 = {hv[p][k][0][2], hv[p][k][0][3], hv[p][k][1][2], hv[p][k][1][3]};
          bf16x8 h0 = __builtin_bit_cast(bf16x8, x0), h1 = __builtin_bit_cast(bf16x8, x1);
#pragma unroll
          for (int a = 0; a < 4; ++a) {
            acc[a][0] = __builtin_amdgcn_mfma_f32_16x16x32_bf16(w2f[k][a], h0, acc[a][0], 0, 0, 0);
            acc[a][1] = __builtin_amdgcn_mfma_f32_16x16x32_bf16(w2f[k][a], h1, acc[a][1], 0, 0, 0);
          }
        }
      }
      __builtin_amdgcn_sched_barrier(0);
#pragma unroll
      for (int a = 0; a < 4; ++a)
#pragma unroll
        for (int t = 0; t < 2; ++t) *(f32x4*)(smem + K4_LDS_R + ((w * 4 + a) * 4 + 2 * p + t) * 1024 + l * 16) = acc[a][t];
      __builtin_amdgcn_sched_barrier(0);
    }
    STAMP(3, 1);
    raw_barrier();
    STAMP(3, 2);
    const float bq[4] = {bflo(b2v[0]), bfhi(b2v[0]), bflo(b2v[1]), bfhi(b2v[1])};
#pragma unroll
    for (int i = 0; i < 2; ++i) {
      const int t = 2 * i + (w & 1);
      f32x4 sacc = *(const f32x4*)(smem + K4_LDS_R + ((0 * 4 + (w >> 1)) * 4 + t) * 1024 + l * 16);
#pragma unroll
      for (int ww = 1; ww < 8; ++ww) sacc += *(const f32x4*)(smem + K4_LDS_R + ((ww * 4 + (w >> 1)) * 4 + t) * 1024 + l * 16);
      float o[4];
      o[0] = bflo(hres[i][0]) + rbf(rbf(sacc[0]) + bq[0]);
      o[1] = bfhi(hres[i][0]) + rbf(rbf(sacc[1]) + bq[1]);
      o[2] = bflo(hres[i][1]) + rbf(rbf(sacc[2]) + bq[2]);
      o[3] = bfhi(hres[i][1]) + rbf(rbf(sacc[3]) + bq[3]);
      u32x2 pk = {pk_bf16(o[0], o[1]), pk_bf16(o[2], o[3])};
      *(u32x2*)(out + (size_t)(tb * 64 + t * 16 + l15) * DM + cg * 64 + (w >> 1) * 16 + 4 * g) = pk;
    }
  }
  STAMP(3, 3);
  STAMP_END(3, 4);
}

extern "C" void kernel_launch(void* const* d_in, const int* in_sizes, int n_in,
                              void* d_out, int out_size, void* d_ws, size_t ws_size,
                              hipStream_t stream) {
  fused_main<<<256, 512, 0, stream>>>((const u16*)d_in[0], (const u16*)d_in[1], (const u16*)d_in[2], (const u16*)d_in[3],
                                      (const u16*)d_in[4], (const u16*)d_in[5], (const u16*)d_in[6], (const u16*)d_in[8],
                                      (const u16*)d_in[9], (const u16*)d_in[10], (const u16*)d_in[11], (char*)d_ws, (u16*)d_out);
}
```

```cpp
#include <hip/hip_runtime.h>
#include <hip/hip_bf16.h>
#include <stdint.h>

typedef unsigned short u16;
typedef __attribute__((ext_vector_type(8))) _Float16 f16x8;
typedef __attribute__((ext_vector_type(2))) _Float16 f16x2;
typedef __attribute__((ext_vector_type(8))) __bf16 bf16x8;
typedef __attribute__((ext_vector_type(2))) __bf16 bf16x2;
typedef __attribute__((ext_vector_type(2))) float f32x2;
typedef __attribute__((ext_vector_type(4))) float f32x4;
typedef __attribute__((ext_vector_type(4))) unsigned u32x4;
typedef __attribute__((ext_vector_type(2))) unsigned u32x2;
typedef __attribute__((ext_vector_type(4))) short s16x4;
#define LDS_AS __attribute__((address_space(3)))

#define SEQ 2048
#define DM 512
#define NM 128
#define FF 1376
#define NFT 86
#define NKS2 43

#define NFTP 88
#define NKSP 44
#define XT_PITCH 4224
#define WS_EPOCH 0
#define WS_FW 1024
#define WS_F1 4096
#define WS_F2 8192
#define WS_FX 12288
#define WS_G0 16384
#define WS_W1T 32768
#define WS_W2T (WS_W1T + NFTP * 16 * 1024)
#define WS_XT (WS_W2T + 32 * NKSP * 1024)
#define WS_HT (WS_XT + DM * XT_PITCH)
#define WS_HACT (WS_HT + DM * XT_PITCH)
#define WS_END (WS_HACT + 64 * NFTP * 1024)

#define STAMP(k, i)
#define STAMP_END(k, i)

__device__ __forceinline__ float bf2f(unsigned v) { return __uint_as_float(v << 16); }
__device__ __forceinline__ float bflo(unsigned v) { return __uint_as_float(v << 16); }
__device__ __forceinline__ float bfhi(unsigned v) { return __uint_as_float(v & 0xffff0000u); }
__device__ __forceinline__ unsigned pk_bf16(float a, float b) {
  f32x2 v = {a, b};
  bf16x2 r = __builtin_convertvector(v, bf16x2);
  return __builtin_bit_cast(unsigned, r);
}
__device__ __forceinline__ unsigned pk_f16(float a, float b) {
  f32x2 v = {a, b};
  f16x2 r = __builtin_convertvector(v, f16x2);
  return __builtin_bit_cast(unsigned, r);
}
__device__ __forceinline__ float rbf(float a) { return bflo(pk_bf16(a, a)); }
__device__ __forceinline__ float h2f_lo(unsigned v) { f16x2 h = __builtin_bit_cast(f16x2, v); return (float)h[0]; }
__device__ __forceinline__ float h2f_hi(unsigned v) { f16x2 h = __builtin_bit_cast(f16x2, v); return (float)h[1]; }
__device__ __forceinline__ unsigned lds_addr(const void* p) { return (unsigned)(size_t)p; }
__device__ __forceinline__ u32x2 tr_read(unsigned ad) {
  s16x4 v = __builtin_amdgcn_ds_read_tr16_b64_v4i16((LDS_AS s16x4*)(size_t)ad);
  return __builtin_bit_cast(u32x2, v);
}
__device__ __forceinline__ void glds16(const void* gsrc, unsigned lds_dst) {
  unsigned keep;
  asm volatile("s_mov_b32 %0, m0\n\ts_mov_b32 m0, %2\n\ts_nop 0\n\tglobal_load_lds_dwordx4 %1, off\n\ts_mov_b32 m0, %0"
               : "=&s"(keep) : "v"(gsrc), "s"(lds_dst) : "memory");
}
__device__ __forceinline__ void glds16_sc1(const void* gsrc, unsigned lds_dst) {
  unsigned keep;
  asm volatile("s_mov_b32 %0, m0\n\ts_mov_b32 m0, %2\n\ts_nop 0\n\tglobal_load_lds_dwordx4 %1, off sc1\n\ts_mov_b32 m0, %0"
               : "=&s"(keep) : "v"(gsrc), "s"(lds_dst) : "memory");
}
__device__ __forceinline__ void store8_dev(void* p, u32x2 v) {
  unsigned long long x = ((unsigned long long)v[1] << 32) | v[0];
  __hip_atomic_store((unsigned long long*)p, x, __ATOMIC_RELAXED, __HIP_MEMORY_SCOPE_AGENT);
}
__device__ __forceinline__ void raw_barrier() {
  asm volatile("s_waitcnt lgkmcnt(0)" ::: "memory");
  __builtin_amdgcn_s_barrier();
  asm volatile("" ::: "memory");
}
template <int CTRL>
__device__ __forceinline__ float dpp_add(float v) {
  int r = __builtin_amdgcn_update_dpp(0, __float_as_int(v), CTRL, 0xf, 0xf, false);
  return v + __int_as_float(r);
}
__device__ __forceinline__ float row16_sum(float v) {
  v = dpp_add<0xB1>(v);
  v = dpp_add<0x4E>(v);
  v = dpp_add<0x141>(v);
  v = dpp_add<0x140>(v);
  return v;
}

struct cpx { float r, i; };
__device__ __forceinline__ cpx cmul(cpx a, cpx b) {
  cpx o; o.r = a.r * b.r - a.i * b.i; o.i = a.r * b.i + a.i * b.r; return o;
}
__device__ __forceinline__ cpx cexp_dir(float lr2, float fr, float e) {
  float mag = __builtin_amdgcn_exp2f(lr2 * e);
  float ph = __builtin_amdgcn_fractf(fr * e);
  cpx o; o.r = mag * __builtin_amdgcn_cosf(ph); o.i = mag * __builtin_amdgcn_sinf(ph); return o;
}

#define K2_ROWB 272
#define K2_COPY (18 * K2_ROWB)
#define K2_UCH (4 * K2_COPY)
#define K2_LDS_U 0
#define K2_LDS_K (39 * 1024)
#define K2_LDS_X (K2_LDS_K + 2 * 4608)
#define KG_KCB 1344
#define KG_LDS_KC (K2_LDS_X + 8192)
#define K2_LDS_Y (KG_LDS_KC + 3072)
#define KG_AROW 528
#define KG_LDS_A K2_LDS_Y
#define KG_LDS_B (KG_LDS_A + 2 * 32 * KG_AROW)
#define P1_LDS_W (KG_LDS_B + 2 * 21 * KG_AROW)
#define P1_LDS_RED (P1_LDS_W + 4 * 5120)
#define K3_LDS_H 0
#define K3_LDS_E 65536
#define K3_LDS_ST (K3_LDS_E + 8 * 9216)
#define K4_LDS_R 0
#define KB_LDS_SIZE (K3_LDS_ST + 1024)
#define SPIN_MAX (1u << 22)

__device__ __forceinline__ f16x8 ld_g(const char* p) {
  u32x2 lo = *(const u32x2*)p, hi = *(const u32x2*)(p + 8);
  u32x4 t = {lo[0], lo[1], hi[0], hi[1]};
  return __builtin_bit_cast(f16x8, t);
}
__device__ __forceinline__ bf16x8 ld_bfrag(unsigned ad) {
  u32x2 l2 = tr_read(ad), h2 = tr_read(ad + 512);
  u32x4 tt = {l2[0], l2[1], h2[0], h2[1]};
  return __builtin_bit_cast(bf16x8, tt);
}

__device__ __forceinline__ float a_chain(char* Aimg, int ch, const float (&lr2)[2], const float (&fr)[2], unsigned v_cr, unsigned v_ci) {
  float cm = fmaxf(fmaxf(fabsf(bflo(v_cr)), fabsf(bfhi(v_cr))), fmaxf(fabsf(bflo(v_ci)), fabsf(bfhi(v_ci))));
#pragma unroll
  for (int o = 32; o > 0; o >>= 1) cm = fmaxf(cm, __shfl_xor(cm, o));
  const float cinv = cm > 0.f ? 1.f / cm : 0.f;
  cpx P[2][2], z16[2];
  const float e0 = 64.f * ch;
#pragma unroll
  for (int m = 0; m < 2; ++m) {
    cpx C; C.r = (m ? bfhi(v_cr) : bflo(v_cr)) * cinv; C.i = (m ? bfhi(v_ci) : bflo(v_ci)) * cinv;
    cpx z8 = cexp_dir(lr2[m], fr[m], 8.f);
    z16[m] = cmul(z8, z8);
    P[m][0] = cmul(C, cexp_dir(lr2[m], fr[m], e0));
    P[m][1] = cmul(P[m][0], z8);
  }
#pragma unroll
  for (int a = 0; a < 4; ++a) {
#pragma unroll
    for (int k = 0; k < 2; ++k) {
      u32x2 o = {pk_f16(P[0][k].r, P[0][k].i), pk_f16(P[1][k].r, P[1][k].i)};
      *(u32x2*)(Aimg + (8 * ch + 2 * a + k) * 528) = o;
      if (a < 3) { P[0][k] = cmul(P[0][k], z16[0]); P[1][k] = cmul(P[1][k], z16[1]); }
    }
  }
  return cm;
}

__device__ __forceinline__ bool flags_ok(u32x4 v, unsigned t0, unsigned t1) {
  return v[0] == t0 && v[1] == t1 && v[2] == t0 && v[3] == t1;
}

extern "C" __global__ __launch_bounds__(512, 2) void fused_main(const u16* __restrict__ x,
                                               const u16* __restrict__ lar, const u16* __restrict__ aim,
                                               const u16* __restrict__ cre, const u16* __restrict__ cim,
                                               const u16* __restrict__ ldt, const u16* __restrict__ dsk,
                                               const u16* __restrict__ w1, const u16* __restrict__ b1,
                                               const u16* __restrict__ w2, const u16* __restrict__ b2,
                                               char* __restrict__ ws, u16* __restrict__ out) {
  __shared__ __attribute__((aligned(16))) char smem[KB_LDS_SIZE];
  const int b = blockIdx.x, tid = threadIdx.x, l = tid & 63;
  const int w = __builtin_amdgcn_readfirstlane(tid >> 6);
  const int g = l >> 4, l15 = l & 15;
  const int cp = (b & 7) * 32 + (b >> 3);
  const int tb = (b & 7) * 4 + ((b >> 3) & 3);
  const int cg = b >> 5;
  const int wl = w & 3, wh = w >> 2;
  const unsigned sb = lds_addr(smem);
  const __amdgpu_buffer_rsrc_t rsy = __builtin_amdgcn_make_buffer_rsrc(ws, 0, 32768, 0x00020000);
  float* red = (float*)(smem + P1_LDS_RED);
  volatile LDS_AS unsigned* lnflag = (volatile LDS_AS unsigned*)(size_t)(sb + P1_LDS_RED + 256);
  STAMP(0, 0);
  const unsigned epoch = __hip_atomic_load((const unsigned*)(ws + WS_EPOCH), __ATOMIC_RELAXED, __HIP_MEMORY_SCOPE_AGENT);
  unsigned xr8[8];
  float piv[8];
  u32x4 wv[2][2];
  const int gw = b * 4 + wl;
  if (wh == 0) {
    const unsigned* xr = (const unsigned*)(x + b * 8 * DM + 128 * wl + 2 * l);
#pragma unroll
    for (int r = 0; r < 8; ++r) xr8[r] = xr[r * 256];
#pragma unroll
    for (int r = 0; r < 8; ++r) piv[r] = bf2f(x[(b * 8 + r) * DM]);
    if (l == 0) lnflag[wl] = 0u;
#pragma unroll
    for (int it = 0; it < 2; ++it) {
      const int bi = gw + it * 1024;
      if (bi < 688) {
        const int ks = bi / 43, fb = bi - ks * 43;
        const u16* src = w1 + (size_t)(32 * ks + (l >> 2)) * FF + 32 * fb + 8 * (l & 3);
        wv[it][0] = *(const u32x4*)src;
        wv[it][1] = *(const u32x4*)(src + 16 * FF);
      } else if (bi < 1376) {
        const int b2 = bi - 688, ks = b2 >> 4, nbk = b2 & 15;
        const u16* src = w2 + (size_t)(32 * ks + (l >> 2)) * DM + 32 * nbk + 8 * (l & 3);
        wv[it][0] = *(const u32x4*)src;
        wv[it][1] = *(const u32x4*)(src + 16 * DM);
      }
    }
  }
  u32x4 xt[2];
  if (wh == 1) {
    const u16* src = x + (size_t)((b >> 3) * 64 + (l >> 1)) * DM + (b & 7) * 64 + wl * 16 + (l & 1) * 8;
    xt[0] = *(const u32x4*)src;
    xt[1] = *(const u32x4*)(src + 32 * DM);
    if (l == 0) lnflag[4 + wl] = 0u;
  }
  const int kc_c = wl >> 1, part = wl & 1;
  const int dch = 2 * cp + kc_c;
  const unsigned v_lar = *(const unsigned*)(lar + dch * NM + 2 * l), v_aim = *(const unsigned*)(aim + dch * NM + 2 * l);
  const unsigned v_cr = *(const unsigned*)(cre + dch * NM + 2 * l), v_ci = *(const unsigned*)(cim + dch * NM + 2 * l);
  const unsigned v_ldt = *(const unsigned*)(ldt + 2 * cp);
  const unsigned dkv = *(const unsigned*)(dsk + 2 * cp);
  if (l < 35) *(u32x4*)(smem + K2_LDS_U + w * K2_COPY + (l < 34 ? l * 16 : 17 * K2_ROWB + 256)) = (u32x4){0u, 0u, 0u, 0u};
  if (wl == 3 && l < 18) *(u32x4*)(smem + K2_LDS_K + wh * 4608 + (l < 3 ? l : 257 + l) * 16) = (u32x4){0u, 0u, 0u, 0u};
  if (wl == 2 && l < 2) *(u32x4*)(smem + KG_LDS_KC + wh * KG_KCB + l * 16) = (u32x4){0u, 0u, 0u, 0u};
  raw_barrier();
  const unsigned tag0 = epoch + 1u, tag1 = tag0 * 0x9E3779B1u + 0x7F4A7C15u;
  const float dt = rbf(__expf(kc_c ? bfhi(v_ldt) : bflo(v_ldt)));
  float lr2[2], fr[2];
  if (wh == 0) {
    float s[16];
#pragma unroll
    for (int r = 0; r < 8; ++r) {
      float a = bflo(xr8[r]) - piv[r], bb = bfhi(xr8[r]) - piv[r];
      s[r] = a + bb;
      s[8 + r] = a * a + bb * bb;
    }
    float t8[8], t4[4];
#pragma unroll
    for (int i = 0; i < 8; ++i) {
      auto sw = __builtin_amdgcn_permlane32_swap(__float_as_uint(s[i]), __float_as_uint(s[8 + i]), false, false);
      t8[i] = __uint_as_float(sw[0]) + __uint_as_float(sw[1]);
    }
#pragma unroll
    for (int i = 0; i < 4; ++i) {
      auto sw = __builtin_amdgcn_permlane16_swap(__float_as_uint(t8[i]), __float_as_uint(t8[4 + i]), false, false);
      t4[i] = row16_sum(__uint_as_float(sw[0]) + __uint_as_float(sw[1]));
    }
    if (l15 == 0) *(f32x4*)(red + wl * 16 + g * 4) = (f32x4){t4[0], t4[1], t4[2], t4[3]};
    asm volatile("s_waitcnt lgkmcnt(0)" ::: "memory");
    if (l == 0) lnflag[wl] = 1u;
    if (wl == 0) {
#pragma unroll 1
      for (unsigned it = 0; it < (1u << 20); ++it) {
        unsigned f = lnflag[l & 3];
        if (__all(f == 1u)) break;
        __builtin_amdgcn_s_sleep(1);
      }
      asm volatile("" ::: "memory");
      const int r = l & 7;
      float t1 = red[r] + red[16 + r] + red[32 + r] + red[48 + r];
      float t2 = red[8 + r] + red[24 + r] + red[40 + r] + red[56 + r];
      float pv = piv[0];
#pragma unroll
      for (int k = 1; k < 8; ++k) pv = r == k ? piv[k] : pv;
      float dm = t1 * (1.f / 512.f);
      float var = fmaxf(t2 * (1.f / 512.f) - dm * dm, 0.f);
      const unsigned md = pk_bf16(pv + dm, __builtin_amdgcn_sqrtf(rbf(rbf(var) + 1e-5f)));
      if (l < 8) store8_dev(ws + WS_G0 + (b * 8 + l) * 8, (u32x2){md, tag0});
    }
    STAMP(0, 1);
    {
      char* wlp = smem + P1_LDS_W + wl * 5120;
      const __amdgpu_buffer_rsrc_t rwt = __builtin_amdgcn_make_buffer_rsrc(ws + WS_W1T, 0, (NFTP * 16 + 32 * NKSP) * 1024, 0x00020000);
      if (wl == 2 && b < 32) __builtin_amdgcn_raw_buffer_store_b128((u32x4){0u, 0u, 0u, 0u}, rwt, (86 * 16 + b) * 1024 + l * 16, 0, 16);
#pragma unroll
      for (int it = 0; it < 2; ++it) {
        const int bi = gw + it * 1024;
        if (bi < 1376) {
          char* t = wlp + it * 2560;
          *(u32x4*)(t + (l >> 2) * 80 + (l & 3) * 16) = wv[it][0];
          *(u32x4*)(t + (16 + (l >> 2)) * 80 + (l & 3) * 16) = wv[it][1];
          const bool isw1 = bi < 688;
          const int q = l15 >> 2, p = l15 & 3;
          const unsigned a0 = lds_addr(t) + (isw1 ? (8 * g + q) : (4 * g + q)) * 80 + p * 8;
          const unsigned dr = isw1 ? 4 * 80 : 16 * 80;
#pragma unroll
          for (int ct = 0; ct < 2; ++ct) {
            u32x2 lo = tr_read(a0 + ct * 32), hi = tr_read(a0 + ct * 32 + dr);
            u32x4 o = {lo[0], lo[1], hi[0], hi[1]};
            unsigned off;
            if (isw1) { const int ks = bi / 43, fb = bi - ks * 43; off = ((2 * fb + ct) * 16 + ks) * 1024; }
            else { const int b2 = bi - 688, ks = b2 >> 4; off = (NFTP * 16 + (2 * (b2 & 15) + ct) * NKSP + ks) * 1024; }
            __builtin_amdgcn_raw_buffer_store_b128(o, rwt, off + l * 16, 0, 16);
          }
        }
      }
    }
    lr2[0] = (-rbf(__expf(bflo(v_lar))) * dt) * 1.4426950408889634f;
    lr2[1] = (-rbf(__expf(bfhi(v_lar))) * dt) * 1.4426950408889634f;
    fr[0] = (bflo(v_aim) * dt) * 0.15915494309189535f;
    fr[1] = (bfhi(v_aim) * dt) * 0.15915494309189535f;
    const float cm = a_chain(smem + KG_LDS_A + kc_c * 32 * KG_AROW + l * 8, 2 * part, lr2, fr, v_cr, v_ci);
    if (part == 0 && l == 0) red[64 + 8 + kc_c] = 2.f * dt * cm * 128.f;
    asm volatile("s_waitcnt vmcnt(0)" ::: "memory");
    STAMP(0, 2);
    raw_barrier();
    if (tid == 192) store8_dev(ws + WS_FW + b * 8, (u32x2){tag0, tag1});
    {
      const char* Ab = smem + KG_LDS_A + kc_c * 32 * KG_AROW + (16 * part + l15) * KG_AROW + g * 16;
      const char* B0 = smem + KG_LDS_B + kc_c * 21 * KG_AROW + l15 * KG_AROW + g * 16;
      const char* B1 = smem + KG_LDS_B + kc_c * 21 * KG_AROW + (16 + (l15 < 5 ? l15 : 4)) * KG_AROW + g * 16;
      f16x8 af[8], b0[8], b1f[8];
#pragma unroll
      for (int q = 0; q < 8; ++q) { af[q] = *(const f16x8*)(Ab + q * 64); b0[q] = *(const f16x8*)(B0 + q * 64); b1f[q] = *(const f16x8*)(B1 + q * 64); }
      f32x4 acc0 = {0, 0, 0, 0}, acc1 = {0, 0, 0, 0};
#pragma unroll
      for (int q = 0; q < 8; ++q) {
        acc0 = __builtin_amdgcn_mfma_f32_16x16x32_f16(af[q], b0[q], acc0, 0, 0, 0);
        acc1 = __builtin_amdgcn_mfma_f32_16x16x32_f16(af[q], b1f[q], acc1, 0, 0, 0);
      }
      _Float16* kc = (_Float16*)(smem + KG_LDS_KC + kc_c * KG_KCB + 32);
      const int a0 = 16 * part + 4 * g;
      int base, stride;
      if (l15 < 8) { base = l15; stride = 8; }
      else if (l15 < 12) { base = 256 + (l15 - 8); stride = 4; }
      else { base = 384 + 64 * ((l15 - 12) >> 1) + (l15 & 1); stride = 2; }
#pragma unroll
      for (int r = 0; r < 4; ++r) {
        kc[base + stride * (a0 + r)] = (_Float16)(acc0[r] * (1.f / 128.f));
        if (l15 < 4 || (l15 == 4 && a0 + r == 0)) kc[512 + 32 * l15 + a0 + r] = (_Float16)(acc1[r] * (1.f / 128.f));
      }
    }
    STAMP(0, 3);
    raw_barrier();
    u32x4 fwa = {0u, 0u, 0u, 0u}, fwb = {0u, 0u, 0u, 0u};
    if (w == 3) {
      fwa = __builtin_amdgcn_raw_buffer_load_b128(rsy, WS_FW + l * 32, 0, 16);
      fwb = __builtin_amdgcn_raw_buffer_load_b128(rsy, WS_FW + l * 32 + 16, 0, 16);
    }
    __builtin_amdgcn_sched_barrier(0);
    {
      const int c2 = tid + 1;
      int ih; unsigned tbl;
      if (c2 <= 32) { ih = 8 * c2; tbl = 0x76543210u; }
      else if (c2 <= 64) { ih = 256 + 4 * (c2 - 32); tbl = 0x43222100u; }
      else if (c2 <= 128) { ih = 384 + 2 * (c2 - 64); tbl = 0x22111000u; }
      else { ih = 512 + (c2 - 128); tbl = 0x11100000u | ((unsigned)(c2 & 1) << 16); }
#pragma unroll
      for (int c = 0; c < 2; ++c) {
        const u16* kc0 = (const u16*)(smem + KG_LDS_KC + c * KG_KCB + 32);
        const u16* kc = kc0 + ih;
        unsigned v[8];
#pragma unroll
        for (int j = 0; j < 8; ++j) v[j] = kc[-(int)((tbl >> (4 * j)) & 15u)];
        if (c2 == 256) v[0] = 0u;
        u32x4 o = {v[0] | (v[1] << 16), v[2] | (v[3] << 16), v[4] | (v[5] << 16), v[6] | (v[7] << 16)};
        *(u32x4*)(smem + K2_LDS_K + c * 4608 + (259 - c2) * 16) = o;
        if (c2 == 1) *(u32x4*)(smem + K2_LDS_K + c * 4608 + 259 * 16) = (u32x4){(unsigned)kc0[0], 0u, 0u, 0u};
      }
    }
    if (w == 3) {
#pragma unroll 1
      for (unsigned it = 0; it < SPIN_MAX; ++it) {
        if (__all(flags_ok(fwa, tag0, tag1) && flags_ok(fwb, tag0, tag1))) break;
        __builtin_amdgcn_s_sleep(2);
        fwa = __builtin_amdgcn_raw_buffer_load_b128(rsy, WS_FW + l * 32, 0, 16);
        fwb = __builtin_amdgcn_raw_buffer_load_b128(rsy, WS_FW + l * 32 + 16, 0, 16);
      }
    }
    STAMP(0, 5);
  } else {
    const int at = tid - 256;
    const __amdgpu_buffer_rsrc_t rxt = __builtin_amdgcn_make_buffer_rsrc(ws + WS_XT, 0, DM * XT_PITCH, 0x00020000);
    {
      char* tl = smem + K2_LDS_X + wl * 2048;
      *(u32x4*)(tl + l * 16) = xt[0];
      *(u32x4*)(tl + 1024 + l * 16) = xt[1];
      const int q = l15 >> 2, p = l15 & 3;
      const unsigned ta = lds_addr(tl) + (8 * g + q) * 32 + p * 8;
      u32x4 pc[2];
#pragma unroll
      for (int i = 0; i < 2; ++i) {
        u32x2 lo = tr_read(ta + i * 1024), hi = tr_read(ta + i * 1024 + 128);
        pc[i] = (u32x4){lo[0], lo[1], hi[0], hi[1]};
      }
      asm volatile("s_waitcnt lgkmcnt(0)" ::: "memory");
#pragma unroll
      for (int i = 0; i < 2; ++i) *(u32x4*)(tl + l15 * 128 + (g + 4 * i) * 16) = pc[i];
#pragma unroll
      for (int i = 0; i < 2; ++i) {
        const u32x4 o = *(const u32x4*)(tl + i * 1024 + l * 16);
        const int ch = (b & 7) * 64 + wl * 16 + i * 8 + (l >> 3);
        __builtin_amdgcn_raw_buffer_store_b128(o, rxt, ch * XT_PITCH + (b >> 3) * 128 + (l & 7) * 16, 0, 16);
      }
      asm volatile("s_waitcnt vmcnt(0)" ::: "memory");
      if (l == 0) lnflag[4 + wl] = 1u;
      if (wl == 0) {
#pragma unroll 1
        for (unsigned it = 0; it < (1u << 20); ++it) {
          unsigned f = lnflag[4 + (l & 3)];
          if (__all(f == 1u)) break;
          __builtin_amdgcn_s_sleep(1);
        }
        if (l == 0) store8_dev(ws + WS_FX + ((b & 7) * 32 + (b >> 3)) * 8, (u32x2){tag0, tag1});
      }
    }
    STAMP(0, 4);
    lr2[0] = (-rbf(__expf(bflo(v_lar))) * dt) * 1.4426950408889634f;
    lr2[1] = (-rbf(__expf(bfhi(v_lar))) * dt) * 1.4426950408889634f;
    fr[0] = (bflo(v_aim) * dt) * 0.15915494309189535f;
    fr[1] = (bfhi(v_aim) * dt) * 0.15915494309189535f;
    {
      char* Bimg = smem + KG_LDS_B + kc_c * 21 * KG_AROW + l * 8;
#define PUTB(col, v0, v1) { cpx _a = (v0), _b = (v1); u32x2 _o = {pk_f16(_a.r, -_a.i), pk_f16(_b.r, -_b.i)}; *(u32x2*)(Bimg + (col) * KG_AROW) = _o; }
      if (part == 0) {
        cpx z[2][8], e256[2];
#pragma unroll
        for (int m = 0; m < 2; ++m) {
          z[m][0].r = 1.f; z[m][0].i = 0.f;
          z[m][1] = cexp_dir(lr2[m], fr[m], 1.f);
          z[m][2] = cmul(z[m][1], z[m][1]); z[m][3] = cmul(z[m][2], z[m][1]); z[m][4] = cmul(z[m][2], z[m][2]);
          z[m][5] = cmul(z[m][4], z[m][1]); z[m][6] = cmul(z[m][4], z[m][2]); z[m][7] = cmul(z[m][4], z[m][3]);
          e256[m] = cexp_dir(lr2[m], fr[m], 256.f);
        }
#pragma unroll
        for (int k = 0; k < 8; ++k) PUTB(k, z[0][k], z[1][k]);
        PUTB(8, e256[0], e256[1]);
#pragma unroll
        for (int k = 1; k < 4; ++k) PUTB(8 + k, cmul(e256[0], z[0][2 * k]), cmul(e256[1], z[1][2 * k]));
      } else {
        cpx z4[2], e256[2], e512[2], e768[2], e1024[2];
#pragma unroll
        for (int m = 0; m < 2; ++m) {
          z4[m] = cexp_dir(lr2[m], fr[m], 4.f);
          e256[m] = cexp_dir(lr2[m], fr[m], 256.f);
          e1024[m] = cexp_dir(lr2[m], fr[m], 1024.f);
          e512[m] = cmul(e256[m], e256[m]); e768[m] = cmul(e512[m], e256[m]);
        }
        PUTB(12, e512[0], e512[1]); PUTB(13, cmul(e512[0], z4[0]), cmul(e512[1], z4[1]));
        PUTB(14, e768[0], e768[1]); PUTB(15, cmul(e768[0], z4[0]), cmul(e768[1], z4[1]));
        PUTB(16, e1024[0], e1024[1]);
        PUTB(17, cmul(e1024[0], e256[0]), cmul(e1024[1], e256[1]));
        PUTB(18, cmul(e1024[0], e512[0]), cmul(e1024[1], e512[1]));
        PUTB(19, cmul(e1024[0], e768[0]), cmul(e1024[1], e768[1]));
        PUTB(20, cmul(e1024[0], e1024[0]), cmul(e1024[1], e1024[1]));
      }
#undef PUTB
    }
    a_chain(smem + KG_LDS_A + kc_c * 32 * KG_AROW + l * 8, 2 * part + 1, lr2, fr, v_cr, v_ci);
    STAMP(0, 1);
    raw_barrier();
    STAMP(0, 2);
    const int p0 = 8 * at;
    const unsigned upos = ((p0 >> 7) + 2) * K2_ROWB + (p0 & 127) * 2;
    const bool dup = (p0 & 127) == 0;
    u32x4 uo[2];
    {
#pragma unroll 1
      for (unsigned it = 0; it < SPIN_MAX; ++it) {
        u32x4 fa = __builtin_amdgcn_raw_buffer_load_b128(rsy, WS_FX + (cp >> 5) * 256 + (l & 15) * 16, 0, 16);
        if (__all(flags_ok(fa, tag0, tag1))) break;
        __builtin_amdgcn_s_sleep(1);
      }
      asm volatile("" ::: "memory");
      u32x4 xq[2];
#pragma unroll
      for (int c = 0; c < 2; ++c) xq[c] = __builtin_amdgcn_raw_buffer_load_b128(rxt, (2 * cp + c) * XT_PITCH + at * 16, 0, 16);
      u32x4 gq[4];
#pragma unroll 1
      for (unsigned it = 0; it < SPIN_MAX; ++it) {
        bool ok = true;
#pragma unroll
        for (int k = 0; k < 4; ++k) {
          gq[k] = __builtin_amdgcn_raw_buffer_load_b128(rsy, WS_G0 + at * 64 + k * 16, 0, 16);
          ok = ok && gq[k][1] == tag0 && gq[k][3] == tag0;
        }
        if (__all(ok)) break;
        __builtin_amdgcn_s_sleep(1);
      }
#pragma unroll
      for (int k = 0; k < 4; ++k) {
        const float m0 = bflo(gq[k][0]), r0 = __builtin_amdgcn_rcpf(bfhi(gq[k][0]));
        const float m1 = bflo(gq[k][2]), r1 = __builtin_amdgcn_rcpf(bfhi(gq[k][2]));
#pragma unroll
        for (int c = 0; c < 2; ++c) {
          unsigned dd = pk_bf16(bflo(xq[c][k]) - m0, bfhi(xq[c][k]) - m1);
          unsigned uu = pk_bf16(bflo(dd) * r0, bfhi(dd) * r1);
          uo[c][k] = pk_f16(bflo(uu), bfhi(uu));
        }
      }
#pragma unroll
      for (int c = 0; c < 2; ++c) {
        *(u32x4*)(smem + K2_LDS_X + c * 4096 + p0 * 2) = xq[c];
        *(u32x4*)(smem + K2_LDS_U + c * K2_UCH + upos) = uo[c];
        if (dup) *(u32x4*)(smem + K2_LDS_U + c * K2_UCH + upos - K2_ROWB + 256) = uo[c];
      }
    }
    STAMP(0, 3);
    raw_barrier();
#pragma unroll
    for (int c = 0; c < 2; ++c) {
      const u32x2 nx = *(const u32x2*)(smem + K2_LDS_U + c * K2_UCH + upos + 16);
      const unsigned d[6] = {uo[c][0], uo[c][1], uo[c][2], uo[c][3], nx[0], nx[1]};
      unsigned e[5];
#pragma unroll
      for (int i = 0; i < 5; ++i) e[i] = __builtin_amdgcn_alignbit(d[i + 1], d[i], 16);
      const u32x4 c1 = {e[0], e[1], e[2], e[3]}, c2 = {d[1], d[2], d[3], d[4]}, c3 = {e[1], e[2], e[3], e[4]};
      char* dst = smem + K2_LDS_U + c * K2_UCH + upos;
      *(u32x4*)(dst + 1 * K2_COPY) = c1;
      *(u32x4*)(dst + 2 * K2_COPY) = c2;
      *(u32x4*)(dst + 3 * K2_COPY) = c3;
      if (dup) {
        *(u32x4*)(dst + 1 * K2_COPY - K2_ROWB + 256) = c1;
        *(u32x4*)(dst + 2 * K2_COPY - K2_ROWB + 256) = c2;
        *(u32x4*)(dst + 3 * K2_COPY - K2_ROWB + 256) = c3;
      }
      if (at == 0) {
        char* dn = smem + K2_LDS_U + c * K2_UCH + K2_ROWB + 124 * 2;
        *(u32x2*)(dn + 1 * K2_COPY) = (u32x2){0u, d[0] << 16};
        *(u32x2*)(dn + 2 * K2_COPY) = (u32x2){0u, d[0]};
        *(u32x2*)(dn + 3 * K2_COPY) = (u32x2){d[0] << 16, e[0]};
      }
    }
    STAMP(0, 5);
  }
  raw_barrier();
  const float ksc = red[64 + 8 + wh];
  const int myft = cg * 11 + 3 * wl;
  bf16x8 wf[3][8];
  const __amdgpu_buffer_rsrc_t rw1 = __builtin_amdgcn_make_buffer_rsrc(ws + WS_W1T, 0, NFTP * 16 * 1024, 0x00020000);
  const unsigned w1o = (myft * 16 + wh * 8) * 1024;
#define LDW1(a, k) __builtin_bit_cast(bf16x8, __builtin_amdgcn_raw_buffer_load_b128(rw1, l * 16, w1o + (a) * 16 * 1024 + (k) * 1024, 16))
  STAMP(1, 1);
  const u32x2 b2v = *(const u32x2*)(b2 + cg * 64 + (w >> 1) * 16 + 4 * g);
  u32x2 b1v[3];
#pragma unroll
  for (int a = 0; a < 3; ++a) {
    const int f = 16 * (myft + a) + 4 * g;
    b1v[a] = *(const u32x2*)(b1 + (f < FF ? f : 0));
  }
  {
    f32x4 acc[8];
#pragma unroll
    for (int i = 0; i < 8; ++i) acc[i] = (f32x4){0, 0, 0, 0};
    const int phi = l15 & 7, nu = l15 >> 3;
    const char* Fb = smem + K2_LDS_K + wh * 4608 + (259 - l15 + g) * 16 + 192 - 64 * wl;
    const char* Gb = smem + K2_LDS_U + wh * K2_UCH + (phi & 3) * K2_COPY + (nu + 2) * K2_ROWB + ((phi & 4) + 8 * g) * 2 + 192 - 64 * wl;
#pragma unroll
    for (int rr = 0; rr < 2; ++rr) {
      f16x8 F[8], G[8];
#pragma unroll
      for (int q = 0; q < 8; ++q) { G[q] = ld_g(Gb - rr * K2_ROWB + q * 2 * K2_ROWB); F[q] = *(const f16x8*)(Fb - rr * 256 - q * 512); }
      __builtin_amdgcn_sched_barrier(0);
#pragma unroll
      for (int k = 0; k < 8; ++k) wf[rr][k] = LDW1(rr, k);
#pragma unroll
      for (int e = 0; e < 8; ++e)
#pragma unroll
        for (int q = 0; q + e < 8; ++q)
          acc[q + e] = __builtin_amdgcn_mfma_f32_16x16x32_f16(G[e], F[q], acc[q + e], 0, 0, 0);
#pragma unroll
      for (int k = 0; k < 8; ++k) {
        __builtin_amdgcn_sched_group_barrier(0x008, 4, 0);
        __builtin_amdgcn_sched_group_barrier(0x020, 1, 0);
      }
      __builtin_amdgcn_sched_barrier(0);
      if (rr == 0 && wl == 0) {
        f16x8 Gm = ld_g(Gb - 2 * K2_ROWB), F8 = *(const f16x8*)(Fb - 8 * 512);
#pragma unroll
        for (int i = 0; i < 7; ++i) acc[i] = __builtin_amdgcn_mfma_f32_16x16x32_f16(Gm, F[i + 1], acc[i], 0, 0, 0);
        acc[7] = __builtin_amdgcn_mfma_f32_16x16x32_f16(Gm, F8, acc[7], 0, 0, 0);
      }
    }
    STAMP(1, 2);
#pragma unroll
    for (int i = 0; i < 8; ++i) *(f32x4*)(smem + K2_LDS_Y + (w * 8 + i) * 1024 + l * 16) = acc[i];
    raw_barrier();
    const float dk = wh ? bfhi(dkv) : bflo(dkv);
    const int tl = 128 * (g >> 1) + 8 * l15 + 4 * (g & 1);
#pragma unroll
    for (int i = 0; i < 2; ++i) {
      const int ti = 2 * wl + i;
      f32x4 o = *(const f32x4*)(smem + K2_LDS_Y + ((wh * 4 + 0) * 8 + ti) * 1024 + l * 16);
#pragma unroll
      for (int k = 1; k < 4; ++k) o += *(const f32x4*)(smem + K2_LDS_Y + ((wh * 4 + k) * 8 + ti) * 1024 + l * 16);
      const int t0 = 256 * ti + tl;
      u32x2 uu = *(const u32x2*)(smem + K2_LDS_U + wh * K2_UCH + ((t0 >> 7) + 2) * K2_ROWB + (t0 & 127) * 2);
      u32x2 xx = *(const u32x2*)(smem + K2_LDS_X + wh * 4096 + t0 * 2);
      u32x2 res;
#pragma unroll
      for (int j = 0; j < 2; ++j) {
        unsigned yy = pk_bf16(o[2 * j] * ksc, o[2 * j + 1] * ksc);
        unsigned ud = pk_bf16(h2f_lo(uu[j]) * dk, h2f_hi(uu[j]) * dk);
        unsigned z = pk_bf16(bflo(yy) + bflo(ud), bfhi(yy) + bfhi(ud));
        res[j] = pk_bf16(bflo(xx[j]) + bflo(z), bfhi(xx[j]) + bfhi(z));
      }
      store8_dev(ws + WS_HT + (size_t)(2 * cp + wh) * XT_PITCH + t0 * 2, res);
    }
  }
  STAMP(1, 3);
  asm volatile("s_waitcnt vmcnt(0)" ::: "memory");
  raw_barrier();
  if (tid == 192) store8_dev(ws + WS_F1 + b * 8, (u32x2){tag0, tag1});
  STAMP(1, 4);
  const int ks0 = w * 5 + (w < 3 ? w : 3);
  bf16x8 w2f[6][4];
  const __amdgpu_buffer_rsrc_t rw2 = __builtin_amdgcn_make_buffer_rsrc(ws + WS_W2T, 0, 32 * NKSP * 1024, 0x00020000);
  const unsigned w2o = ((cg * 4) * NKSP + ks0) * 1024;
#define LDW2(k, a) __builtin_bit_cast(bf16x8, __builtin_amdgcn_raw_buffer_load_b128(rw2, l * 16, w2o + ((a) * NKSP + (k)) * 1024, 16))
  if (wl < 3) {
#pragma unroll
    for (int k = 0; k < 8; ++k) wf[2][k] = LDW1(2, k);
  } else {
#pragma unroll
    for (int k = 0; k < 8; ++k) wf[2][k] = wf[1][k];
  }
  __builtin_amdgcn_sched_barrier(0);
  const u32x4 onesu = {0x3f803f80u, 0x3f803f80u, 0x3f803f80u, 0x3f803f80u};
  const bf16x8 onesf = __builtin_bit_cast(bf16x8, onesu);
  {
    f32x4 cq[2];
#pragma unroll
    for (int a = 0; a < 2; ++a) cq[a] = (f32x4){0, 0, 0, 0};
#pragma unroll
    for (int k = 0; k < 8; ++k)
#pragma unroll
      for (int a = 0; a < 2; ++a) cq[a] = __builtin_amdgcn_mfma_f32_16x16x32_bf16(wf[a][k], onesf, cq[a], 0, 0, 0);
#pragma unroll
    for (int a = 0; a < 2; ++a) *(f32x4*)(smem + K3_LDS_E + (w * 9 + 6 + a) * 1024 + l * 16) = cq[a];
  }
  STAMP(1, 5);
  if (w == 3) {
#pragma unroll 1
    for (unsigned it = 0; it < SPIN_MAX; ++it) {
      u32x4 fa = __builtin_amdgcn_raw_buffer_load_b128(rsy, WS_F1 + l * 32, 0, 16);
      u32x4 fb = __builtin_amdgcn_raw_buffer_load_b128(rsy, WS_F1 + l * 32 + 16, 0, 16);
      if (__all(flags_ok(fa, tag0, tag1) && flags_ok(fb, tag0, tag1))) break;
      __builtin_amdgcn_s_sleep(1);
    }
    if (b == 0 && l == 0) __hip_atomic_store((unsigned*)(ws + WS_EPOCH), tag0, __ATOMIC_RELAXED, __HIP_MEMORY_SCOPE_AGENT);
  }
  raw_barrier();
  STAMP(2, 0);
  asm volatile("" : "+v"(wf[2][0]), "+v"(wf[2][1]), "+v"(wf[2][2]), "+v"(wf[2][3]), "+v"(wf[2][4]), "+v"(wf[2][5]), "+v"(wf[2][6]), "+v"(wf[2][7]) : "v"(b2v), "v"(b1v[0]), "v"(b1v[1]), "v"(b1v[2]) : "memory");
#pragma unroll
  for (int it = 0; it < 8; ++it) {
    const int j = it * 8 + w;
    const int row = j * 8 + (l >> 3), pc = l & 7;
    const int swz = ((row >> 1) & 1) | (((row >> 3) & 1) << 1);
    const int lc = (((pc >> 1) ^ swz) << 1) | (pc & 1);
    glds16_sc1(ws + WS_HT + (size_t)row * XT_PITCH + tb * 128 + lc * 16, sb + K3_LDS_H + j * 1024);
  }
  {
    f32x4 cq = {0, 0, 0, 0};
#pragma unroll
    for (int k = 0; k < 8; ++k) cq = __builtin_amdgcn_mfma_f32_16x16x32_bf16(wf[2][k], onesf, cq, 0, 0, 0);
    *(f32x4*)(smem + K3_LDS_E + (w * 9 + 8) * 1024 + l * 16) = cq;
  }
  asm volatile("s_waitcnt vmcnt(0)" ::: "memory");
  raw_barrier();
  STAMP(2, 1);
  const int q4 = l15 >> 2, p4 = l15 & 3;
  const __amdgpu_buffer_rsrc_t rs = __builtin_amdgcn_make_buffer_rsrc(ws + WS_HACT, 0, 64 * NFTP * 1024, 0x00020000);
  u32x2 hres[2];
  {
    const int swz = ((q4 >> 1) & 1) | ((g & 1) << 1);
    const unsigned lbase = sb + K3_LDS_H + wh * 8 * 4096 + (8 * g + q4) * 128 + p4 * 8;
    unsigned ad[4];
#pragma unroll
    for (int t = 0; t < 4; ++t) ad[t] = lbase + (((t ^ (2 * wh)) ^ swz) << 5);
    const unsigned sa = lbase + ((wl ^ swz) << 5);
    const unsigned vo2 = wl < 3 ? (unsigned)(l * 16) : 0x7f000000u;
    f32x4 acc[3][2];
    bf16x8 bf[2][2];
    auto finish = [&](int p) {
      f32x4 cs[3], bb[3];
      f32x2 st[2];
#pragma unroll
      for (int a = 0; a < 3; ++a) {
        bb[a] = (f32x4){bflo(b1v[a][0]), bfhi(b1v[a][0]), bflo(b1v[a][1]), bfhi(b1v[a][1])};
        cs[a] = *(const f32x4*)(smem + K3_LDS_E + (w * 9 + 6 + a) * 1024 + l * 16) + *(const f32x4*)(smem + K3_LDS_E + ((w ^ 4) * 9 + 6 + a) * 1024 + l * 16);
      }
#pragma unroll
      for (int t = 0; t < 2; ++t) {
        const int tr = (2 * p + t) * 16 + l15;
        f32x2 p0 = *(const f32x2*)(smem + K3_LDS_ST + tr * 8), p1 = *(const f32x2*)(smem + K3_LDS_ST + (64 + tr) * 8);
        float mean = (p0[0] + p1[0]) * (1.f / 512.f);
        float var = fmaxf((p0[1] + p1[1]) * (1.f / 512.f) - mean * mean, 0.f);
        float m1 = rbf(mean);
        float r1 = __builtin_amdgcn_rcpf(rbf(__builtin_amdgcn_sqrtf(rbf(rbf(var) + 1e-5f))));
        float mean2 = (mean - m1) * r1, var2 = var * r1 * r1;
        float m2 = rbf(mean2);
        float r2 = __builtin_amdgcn_rcpf(rbf(__builtin_amdgcn_sqrtf(rbf(rbf(var2) + 1e-5f))));
        st[t] = (f32x2){r1 * r2, -(m1 * r1 + m2) * r2};
      }
#pragma unroll
      for (int a = 0; a < 3; ++a)
#pragma unroll
        for (int t = 0; t < 2; ++t) acc[a][t] += *(const f32x4*)(smem + K3_LDS_E + ((w ^ 4) * 9 + a * 2 + t) * 1024 + l * 16);
      const unsigned so = ((tb * 2 + p) * NFTP + myft) * 1024;
      const f32x2 c1 = {-0.10294324f, -0.10294324f}, c0 = {-2.30220819f, -2.30220819f}, one = {1.f, 1.f};
      f32x2 v[12], q[12];
#pragma unroll
      for (int i = 0; i < 12; ++i) {
        const int a = i >> 2, t = (i >> 1) & 1, r = 2 * (i & 1);
        const f32x2 a2 = {st[t][0], st[t][0]}, b2 = {st[t][1], st[t][1]};
        v[i] = __builtin_elementwise_fma(a2, (f32x2){acc[a][t][r], acc[a][t][r + 1]},
                                         __builtin_elementwise_fma(b2, (f32x2){cs[a][r], cs[a][r + 1]}, (f32x2){bb[a][r], bb[a][r + 1]}));
      }
      __builtin_amdgcn_sched_barrier(0);
#pragma unroll
      for (int i = 0; i < 12; ++i) q[i] = v[i] * v[i];
      __builtin_amdgcn_sched_barrier(0);
#pragma unroll
      for (int i = 0; i < 12; ++i) q[i] = __builtin_elementwise_fma(q[i], c1, c0);
      __builtin_amdgcn_sched_barrier(0);
#pragma unroll
      for (int i = 0; i < 12; ++i) q[i] = q[i] * v[i];
      __builtin_amdgcn_sched_barrier(0);
#pragma unroll
      for (int i = 0; i < 12; ++i) q[i] = (f32x2){__builtin_amdgcn_exp2f(q[i][0]), __builtin_amdgcn_exp2f(q[i][1])};
      __builtin_amdgcn_sched_barrier(0);
#pragma unroll
      for (int i = 0; i < 12; ++i) q[i] = q[i] + one;
      __builtin_amdgcn_sched_barrier(0);
#pragma unroll
      for (int i = 0; i < 12; ++i) q[i] = (f32x2){__builtin_amdgcn_rcpf(q[i][0]), __builtin_amdgcn_rcpf(q[i][1])};
      __builtin_amdgcn_sched_barrier(0);
#pragma unroll
      for (int i = 0; i < 12; ++i) v[i] = v[i] * q[i];
      __builtin_amdgcn_sched_barrier(0);
#pragma unroll
      for (int a = 0; a < 3; ++a) {
        u32x4 o = {pk_bf16(v[4 * a][0], v[4 * a][1]), pk_bf16(v[4 * a + 1][0], v[4 * a + 1][1]),
                   pk_bf16(v[4 * a + 2][0], v[4 * a + 2][1]), pk_bf16(v[4 * a + 3][0], v[4 * a + 3][1])};
        __builtin_amdgcn_raw_buffer_store_b128(o, rs, (a < 2 ? (unsigned)(l * 16) : vo2) + so + a * 1024, 0, 16);
      }
      __builtin_amdgcn_sched_barrier(0);
    };
    {
      f32x4 sS = {0, 0, 0, 0}, sQ = {0, 0, 0, 0};
      f32x4 acs[3][2];
      bf16x8 bg[2][2], sf[2];
#pragma unroll
      for (int a = 0; a < 3; ++a)
#pragma unroll
        for (int t = 0; t < 2; ++t) { acc[a][t] = (f32x4){0, 0, 0, 0}; acs[a][t] = (f32x4){0, 0, 0, 0}; }
      bf[0][0] = ld_bfrag(ad[0]); bf[0][1] = ld_bfrag(ad[1]); bg[0][0] = ld_bfrag(ad[2]); bg[0][1] = ld_bfrag(ad[3]); sf[0] = ld_bfrag(sa);
      __builtin_amdgcn_sched_barrier(0);
#pragma unroll
      for (int ks = 0; ks < 8; ++ks) {
        const int cur = ks & 1, nx = cur ^ 1;
        if (ks < 7) {
          bf[nx][0] = ld_bfrag(ad[0] + (ks + 1) * 4096); bf[nx][1] = ld_bfrag(ad[1] + (ks + 1) * 4096);
          bg[nx][0] = ld_bfrag(ad[2] + (ks + 1) * 4096); bg[nx][1] = ld_bfrag(ad[3] + (ks + 1) * 4096);
          sf[nx] = ld_bfrag(sa + (ks + 1) * 4096);
        }
        w2f[ks >> 1][(ks & 1) * 2] = LDW2(ks >> 1, (ks & 1) * 2);
        w2f[ks >> 1][(ks & 1) * 2 + 1] = LDW2(ks >> 1, (ks & 1) * 2 + 1);
#pragma unroll
        for (int t = 0; t < 2; ++t)
#pragma unroll
          for (int a = 0; a < 3; ++a) acs[a][t] = __builtin_amdgcn_mfma_f32_16x16x32_bf16(wf[a][ks], bg[cur][t], acs[a][t], 0, 0, 0);
#pragma unroll
        for (int t = 0; t < 2; ++t)
#pragma unroll
          for (int a = 0; a < 3; ++a) acc[a][t] = __builtin_amdgcn_mfma_f32_16x16x32_bf16(wf[a][ks], bf[cur][t], acc[a][t], 0, 0, 0);
        sS = __builtin_amdgcn_mfma_f32_16x16x32_bf16(onesf, sf[cur], sS, 0, 0, 0);
        sQ = __builtin_amdgcn_mfma_f32_16x16x32_bf16(sf[cur], sf[cur], sQ, 0, 0, 0);
#pragma unroll
        for (int k = 0; k < 10; ++k) {
          __builtin_amdgcn_sched_group_barrier(0x008, 1, 0);
          if (ks < 7) __builtin_amdgcn_sched_group_barrier(0x100, 1, 0);
          if (k == 0 || k == 6) __builtin_amdgcn_sched_group_barrier(0x020, 1, 0);
        }
        __builtin_amdgcn_sched_barrier(0);
      }
      STAMP(2, 2);
#pragma unroll
      for (int a = 0; a < 3; ++a)
#pragma unroll
        for (int t = 0; t < 2; ++t) *(f32x4*)(smem + K3_LDS_E + (w * 9 + a * 2 + t) * 1024 + l * 16) = acs[a][t];
      float dq = (l15 & 2) ? ((l15 & 1) ? sQ[3] : sQ[2]) : ((l15 & 1) ? sQ[1] : sQ[0]);
      float s2 = __shfl(dq, l15 + 16 * (l15 >> 2));
      if (l < 16) *(f32x2*)(smem + K3_LDS_ST + (wh * 64 + wl * 16 + l) * 8) = (f32x2){sS[0], s2};
    }
#pragma unroll
    for (int a = 0; a < 4; ++a) w2f[4][a] = LDW2(4, a);
    if (w < 3) {
#pragma unroll
      for (int a = 0; a < 4; ++a) w2f[5][a] = LDW2(5, a);
    }
    {
      const int row = cg * 64 + (w >> 1) * 16 + 4 * g + q4;
      const int swz2 = ((row >> 1) & 1) | (((row >> 3) & 1) << 1);
      const unsigned hb = sb + K3_LDS_H + row * 128 + p4 * 8;
#pragma unroll
      for (int i = 0; i < 2; ++i) hres[i] = tr_read(hb + (((2 * i + (w & 1)) ^ swz2) << 5));
    }
    raw_barrier();
    STAMP(2, 3);
    finish(wh);
  }
  STAMP(2, 4);
  asm volatile("s_waitcnt vmcnt(0)" ::: "memory");
  raw_barrier();
  STAMP(2, 5);
  if (w == 0) {
    if (l == 0) store8_dev(ws + WS_F2 + tb * 128 + cg * 8, (u32x2){tag0, tag1});
#pragma unroll 1
    for (unsigned it = 0; it < SPIN_MAX; ++it) {
      u32x4 fa = __builtin_amdgcn_raw_buffer_load_b128(rsy, WS_F2 + tb * 128 + (l & 3) * 16, 0, 16);
      if (__all(flags_ok(fa, tag0, tag1))) break;
      __builtin_amdgcn_s_sleep(1);
    }
  }
  raw_barrier();
  STAMP(3, 0);
  {
    const unsigned hvo = ((tb * 2) * NFTP + ks0 * 2) * 1024;
    u32x4 hv[2][6][2];
#pragma unroll
    for (int p = 0; p < 2; ++p) {
#pragma unroll
      for (int k = 0; k < 5; ++k)
#pragma unroll
        for (int f = 0; f < 2; ++f)
          hv[p][k][f] = __builtin_amdgcn_raw_buffer_load_b128(rs, l * 16, hvo + (p * NFTP + 2 * k + f) * 1024, 16);
    }
    if (w < 3) {
#pragma unroll
      for (int p = 0; p < 2; ++p)
#pragma unroll
        for (int f = 0; f < 2; ++f)
          hv[p][5][f] = __builtin_amdgcn_raw_buffer_load_b128(rs, l * 16, hvo + (p * NFTP + 10 + f) * 1024, 16);
    }
    __builtin_amdgcn_sched_barrier(0);
#pragma unroll
    for (int p = 0; p < 2; ++p) {
      f32x4 acc[4][2];
#pragma unroll
      for (int a = 0; a < 4; ++a) { acc[a][0] = (f32x4){0, 0, 0, 0}; acc[a][1] = (f32x4){0, 0, 0, 0}; }
#pragma unroll
      for (int k = 0; k < 6; ++k) {
        if (k < 5 || w < 3) {
          u32x4 x0 = {hv[p][k][0][0], hv[p][k][0][1], hv[p][k][1][0], hv[p][k][1][1]};
          u32x4 x1 = {hv[p][k][0][2], hv[p][k][0][3], hv[p][k][1][2], hv[p][k][1][3]};
          bf16x8 h0 = __builtin_bit_cast(bf16x8, x0), h1 = __builtin_bit_cast(bf16x8, x1);
#pragma unroll
          for (int a = 0; a < 4; ++a) {
            acc[a][0] = __builtin_amdgcn_mfma_f32_16x16x32_bf16(w2f[k][a], h0, acc[a][0], 0, 0, 0);
            acc[a][1] = __builtin_amdgcn_mfma_f32_16x16x32_bf16(w2f[k][a], h1, acc[a][1], 0, 0, 0);
          }
        }
      }
      __builtin_amdgcn_sched_barrier(0);
#pragma unroll
      for (int a = 0; a < 4; ++a)
#pragma unroll
        for (int t = 0; t < 2; ++t) *(f32x4*)(smem + K4_LDS_R + ((w * 4 + a) * 4 + 2 * p + t) * 1024 + l * 16) = acc[a][t];
      __builtin_amdgcn_sched_barrier(0);
    }
    STAMP(3, 1);
    raw_barrier();
    STAMP(3, 2);
    const float bq[4] = {bflo(b2v[0]), bfhi(b2v[0]), bflo(b2v[1]), bfhi(b2v[1])};
#pragma unroll
    for (int i = 0; i < 2; ++i) {
      const int t = 2 * i + (w & 1);
      f32x4 sacc = *(const f32x4*)(smem + K4_LDS_R + ((0 * 4 + (w >> 1)) * 4 + t) * 1024 + l * 16);
#pragma unroll
      for (int ww = 1; ww < 8; ++ww) sacc += *(const f32x4*)(smem + K4_LDS_R + ((ww * 4 + (w >> 1)) * 4 + t) * 1024 + l * 16);
      float o[4];
      o[0] = bflo(hres[i][0]) + rbf(rbf(sacc[0]) + bq[0]);
      o[1] = bfhi(hres[i][0]) + rbf(rbf(sacc[1]) + bq[1]);
      o[2] = bflo(hres[i][1]) + rbf(rbf(sacc[2]) + bq[2]);
      o[3] = bfhi(hres[i][1]) + rbf(rbf(sacc[3]) + bq[3]);
      u32x2 pk = {pk_bf16(o[0], o[1]), pk_bf16(o[2], o[3])};
      *(u32x2*)(out + (size_t)(tb * 64 + t * 16 + l15) * DM + cg * 64 + (w >> 1) * 16 + 4 * g) = pk;
    }
  }
  STAMP(3, 3);
  STAMP_END(3, 4);
}

extern "C" void kernel_launch(void* const* d_in, const int* in_sizes, int n_in,
                              void* d_out, int out_size, void* d_ws, size_t ws_size,
                              hipStream_t stream) {
  fused_main<<<256, 512, 0, stream>>>((const u16*)d_in[0], (const u16*)d_in[1], (const u16*)d_in[2], (const u16*)d_in[3],
                                      (const u16*)d_in[4], (const u16*)d_in[5], (const u16*)d_in[6], (const u16*)d_in[8],
                                      (const u16*)d_in[9], (const u16*)d_in[10], (const u16*)d_in[11], (char*)d_ws, (u16*)d_out);
}
```

```cpp
#include <hip/hip_runtime.h>
#include <hip/hip_bf16.h>
#include <stdint.h>

typedef unsigned short u16;
typedef __attribute__((ext_vector_type(8))) _Float16 f16x8;
typedef __attribute__((ext_vector_type(2))) _Float16 f16x2;
typedef __attribute__((ext_vector_type(8))) __bf16 bf16x8;
typedef __attribute__((ext_vector_type(2))) __bf16 bf16x2;
typedef __attribute__((ext_vector_type(2))) float f32x2;
typedef __attribute__((ext_vector_type(4))) float f32x4;
typedef __attribute__((ext_vector_type(4))) unsigned u32x4;
typedef __attribute__((ext_vector_type(2))) unsigned u32x2;
typedef __attribute__((ext_vector_type(4))) short s16x4;
#define LDS_AS __attribute__((address_space(3)))

#define SEQ 2048
#define DM 512
#define NM 128
#define FF 1376
#define NFT 86
#define NKS2 43

#define NFTP 88
#define NKSP 44
#define XT_PITCH 4224
#define WS_EPOCH 0
#define WS_FW 1024
#define WS_F1 4096
#define WS_F2 8192
#define WS_FX 12288
#define WS_G0 16384
#define WS_W1T 32768
#define WS_W2T (WS_W1T + NFTP * 16 * 1024)
#define WS_XT (WS_W2T + 32 * NKSP * 1024)
#define WS_HT (WS_XT + DM * XT_PITCH)
#define WS_HACT (WS_HT + DM * XT_PITCH)
#define WS_END (WS_HACT + 64 * NFTP * 1024)

#define STAMP(k, i)
#define STAMP_END(k, i)

__device__ __forceinline__ float bf2f(unsigned v) { return __uint_as_float(v << 16); }
__device__ __forceinline__ float bflo(unsigned v) { return __uint_as_float(v << 16); }
__device__ __forceinline__ float bfhi(unsigned v) { return __uint_as_float(v & 0xffff0000u); }
__device__ __forceinline__ unsigned pk_bf16(float a, float b) {
  f32x2 v = {a, b};
  bf16x2 r = __builtin_convertvector(v, bf16x2);
  return __builtin_bit_cast(unsigned, r);
}
__device__ __forceinline__ unsigned pk_f16(float a, float b) {
  f32x2 v = {a, b};
  f16x2 r = __builtin_convertvector(v, f16x2);
  return __builtin_bit_cast(unsigned, r);
}
__device__ __forceinline__ float rbf(float a) { return bflo(pk_bf16(a, a)); }
__device__ __forceinline__ float h2f_lo(unsigned v) { f16x2 h = __builtin_bit_cast(f16x2, v); return (float)h[0]; }
__device__ __forceinline__ float h2f_hi(unsigned v) { f16x2 h = __builtin_bit_cast(f16x2, v); return (float)h[1]; }
__device__ __forceinline__ unsigned lds_addr(const void* p) { return (unsigned)(size_t)p; }
__device__ __forceinline__ u32x2 tr_read(unsigned ad) {
  s16x4 v = __builtin_amdgcn_ds_read_tr16_b64_v4i16((LDS_AS s16x4*)(size_t)ad);
  return __builtin_bit_cast(u32x2, v);
}
__device__ __forceinline__ void glds16(const void* gsrc, unsigned lds_dst) {
  unsigned keep;
  asm volatile("s_mov_b32 %0, m0\n\ts_mov_b32 m0, %2\n\ts_nop 0\n\tglobal_load_lds_dwordx4 %1, off\n\ts_mov_b32 m0, %0"
               : "=&s"(keep) : "v"(gsrc), "s"(lds_dst) : "memory");
}
__device__ __forceinline__ void glds16_sc1(const void* gsrc, unsigned lds_dst) {
  unsigned keep;
  asm volatile("s_mov_b32 %0, m0\n\ts_mov_b32 m0, %2\n\ts_nop 0\n\tglobal_load_lds_dwordx4 %1, off sc1\n\ts_mov_b32 m0, %0"
               : "=&s"(keep) : "v"(gsrc), "s"(lds_dst) : "memory");
}
__device__ __forceinline__ void store8_dev(void* p, u32x2 v) {
  unsigned long long x = ((unsigned long long)v[1] << 32) | v[0];
  __hip_atomic_store((unsigned long long*)p, x, __ATOMIC_RELAXED, __HIP_MEMORY_SCOPE_AGENT);
}
__device__ __forceinline__ void raw_barrier() {
  asm volatile("s_waitcnt lgkmcnt(0)" ::: "memory");
  __builtin_amdgcn_s_barrier();
  asm volatile("" ::: "memory");
}
template <int CTRL>
__device__ __forceinline__ float dpp_add(float v) {
  int r = __builtin_amdgcn_update_dpp(0, __float_as_int(v), CTRL, 0xf, 0xf, false);
  return v + __int_as_float(r);
}
__device__ __forceinline__ float row16_sum(float v) {
  v = dpp_add<0xB1>(v);
  v = dpp_add<0x4E>(v);
  v = dpp_add<0x141>(v);
  v = dpp_add<0x140>(v);
  return v;
}

struct cpx { float r, i; };
__device__ __forceinline__ cpx cmul(cpx a, cpx b) {
  cpx o; o.r = a.r * b.r - a.i * b.i; o.i = a.r * b.i + a.i * b.r; return o;
}
__device__ __forceinline__ cpx cexp_dir(float lr2, float fr, float e) {
  float mag = __builtin_amdgcn_exp2f(lr2 * e);
  float ph = __builtin_amdgcn_fractf(fr * e);
  cpx o; o.r = mag * __builtin_amdgcn_cosf(ph); o.i = mag * __builtin_amdgcn_sinf(ph); return o;
}

#define K2_ROWB 272
#define K2_COPY (18 * K2_ROWB)
#define K2_UCH (4 * K2_COPY)
#define K2_LDS_U 0
#define K2_LDS_K (39 * 1024)
#define K2_LDS_X (K2_LDS_K + 2 * 4608)
#define KG_KCB 1344
#define KG_LDS_KC (K2_LDS_X + 8192)
#define K2_LDS_Y (KG_LDS_KC + 3072)
#define KG_AROW 528
#define KG_LDS_A K2_LDS_Y
#define KG_LDS_B (KG_LDS_A + 2 * 32 * KG_AROW)
#define P1_LDS_W (KG_LDS_B + 2 * 21 * KG_AROW)
#define P1_LDS_RED (P1_LDS_W + 4 * 5120)
#define K3_LDS_H 0
#define K3_LDS_E 65536
#define K3_LDS_ST (K3_LDS_E + 8 * 9216)
#define K3_LDS_SF (K3_LDS_ST + 1024)
#define K4_LDS_R 0
#define KB_LDS_SIZE (K3_LDS_SF + 64)
#define SPIN_MAX (1u << 22)

__device__ __forceinline__ f16x8 ld_g(const char* p) {
  u32x2 lo = *(const u32x2*)p, hi = *(const u32x2*)(p + 8);
  u32x4 t = {lo[0], lo[1], hi[0], hi[1]};
  return __builtin_bit_cast(f16x8, t);
}
__device__ __forceinline__ bf16x8 ld_bfrag(unsigned ad) {
  u32x2 l2 = tr_read(ad), h2 = tr_read(ad + 512);
  u32x4 tt = {l2[0], l2[1], h2[0], h2[1]};
  return __builtin_bit_cast(bf16x8, tt);
}

__device__ __forceinline__ float a_chain(char* Aimg, int ch, const float (&lr2)[2], const float (&fr)[2], unsigned v_cr, unsigned v_ci) {
  float cm = fmaxf(fmaxf(fabsf(bflo(v_cr)), fabsf(bfhi(v_cr))), fmaxf(fabsf(bflo(v_ci)), fabsf(bfhi(v_ci))));
#pragma unroll
  for (int o = 32; o > 0; o >>= 1) cm = fmaxf(cm, __shfl_xor(cm, o));
  const float cinv = cm > 0.f ? 1.f / cm : 0.f;
  cpx P[2][2], z16[2];
  const float e0 = 64.f * ch;
#pragma unroll
  for (int m = 0; m < 2; ++m) {
    cpx C; C.r = (m ? bfhi(v_cr) : bflo(v_cr)) * cinv; C.i = (m ? bfhi(v_ci) : bflo(v_ci)) * cinv;
    cpx z8 = cexp_dir(lr2[m], fr[m], 8.f);
    z16[m] = cmul(z8, z8);
    P[m][0] = cmul(C, cexp_dir(lr2[m], fr[m], e0));
    P[m][1] = cmul(P[m][0], z8);
  }
#pragma unroll
  for (int a = 0; a < 4; ++a) {
#pragma unroll
    for (int k = 0; k < 2; ++k) {
      u32x2 o = {pk_f16(P[0][k].r, P[0][k].i), pk_f16(P[1][k].r, P[1][k].i)};
      *(u32x2*)(Aimg + (8 * ch + 2 * a + k) * 528) = o;
      if (a < 3) { P[0][k] = cmul(P[0][k], z16[0]); P[1][k] = cmul(P[1][k], z16[1]); }
    }
  }
  return cm;
}

__device__ __forceinline__ bool flags_ok(u32x4 v, unsigned t0, unsigned t1) {
  return v[0] == t0 && v[1] == t1 && v[2] == t0 && v[3] == t1;
}

extern "C" __global__ __launch_bounds__(512, 2) void fused_main(const u16* __restrict__ x,
                                               const u16* __restrict__ lar, const u16* __restrict__ aim,
                                               const u16* __restrict__ cre, const u16* __restrict__ cim,
                                               const u16* __restrict__ ldt, const u16* __restrict__ dsk,
                                               const u16* __restrict__ w1, const u16* __restrict__ b1,
                                               const u16* __restrict__ w2, const u16* __restrict__ b2,
                                               char* __restrict__ ws, u16* __restrict__ out) {
  __shared__ __attribute__((aligned(16))) char smem[KB_LDS_SIZE];
  const int b = blockIdx.x, tid = threadIdx.x, l = tid & 63;
  const int w = __builtin_amdgcn_readfirstlane(tid >> 6);
  const int g = l >> 4, l15 = l & 15;
  const int cp = (b & 7) * 32 + (b >> 3);
  const int tb = (b & 7) * 4 + ((b >> 3) & 3);
  const int cg = b >> 5;
  const int wl = w & 3, wh = w >> 2;
  const unsigned sb = lds_addr(smem);
  const __amdgpu_buffer_rsrc_t rsy = __builtin_amdgcn_make_buffer_rsrc(ws, 0, 32768, 0x00020000);
  float* red = (float*)(smem + P1_LDS_RED);
  volatile LDS_AS unsigned* lnflag = (volatile LDS_AS unsigned*)(size_t)(sb + P1_LDS_RED + 256);
  STAMP(0, 0);
  const unsigned epoch = __hip_atomic_load((const unsigned*)(ws + WS_EPOCH), __ATOMIC_RELAXED, __HIP_MEMORY_SCOPE_AGENT);
  unsigned xr8[8];
  float piv[8];
  u32x4 wv[2][2];
  const int gw = b * 4 + wl;
  if (wh == 0) {
    const unsigned* xr = (const unsigned*)(x + b * 8 * DM + 128 * wl + 2 * l);
#pragma unroll
    for (int r = 0; r < 8; ++r) xr8[r] = xr[r * 256];
#pragma unroll
    for (int r = 0; r < 8; ++r) piv[r] = bf2f(x[(b * 8 + r) * DM]);
    if (l == 0) lnflag[wl] = 0u;
#pragma unroll
    for (int it = 0; it < 2; ++it) {
      const int bi = gw + it * 1024;
      if (bi < 688) {
        const int ks = bi / 43, fb = bi - ks * 43;
        const u16* src = w1 + (size_t)(32 * ks + (l >> 2)) * FF + 32 * fb + 8 * (l & 3);
        wv[it][0] = *(const u32x4*)src;
        wv[it][1] = *(const u32x4*)(src + 16 * FF);
      } else if (bi < 1376) {
        const int b2 = bi - 688, ks = b2 >> 4, nbk = b2 & 15;
        const u16* src = w2 + (size_t)(32 * ks + (l >> 2)) * DM + 32 * nbk + 8 * (l & 3);
        wv[it][0] = *(const u32x4*)src;
        wv[it][1] = *(const u32x4*)(src + 16 * DM);
      }
    }
  }
  u32x4 xt[2];
  if (wh == 1) {
    const u16* src = x + (size_t)((b >> 3) * 64 + (l >> 1)) * DM + (b & 7) * 64 + wl * 16 + (l & 1) * 8;
    xt[0] = *(const u32x4*)src;
    xt[1] = *(const u32x4*)(src + 32 * DM);
    if (l == 0) lnflag[4 + wl] = 0u;
  }
  const int kc_c = wl >> 1, part = wl & 1;
  const int dch = 2 * cp + kc_c;
  const unsigned v_lar = *(const unsigned*)(lar + dch * NM + 2 * l), v_aim = *(const unsigned*)(aim + dch * NM + 2 * l);
  const unsigned v_cr = *(const unsigned*)(cre + dch * NM + 2 * l), v_ci = *(const unsigned*)(cim + dch * NM + 2 * l);
  const unsigned v_ldt = *(const unsigned*)(ldt + 2 * cp);
  const unsigned dkv = *(const unsigned*)(dsk + 2 * cp);
  if (l < 35) *(u32x4*)(smem + K2_LDS_U + w * K2_COPY + (l < 34 ? l * 16 : 17 * K2_ROWB + 256)) = (u32x4){0u, 0u, 0u, 0u};
  if (wl == 3 && l < 18) *(u32x4*)(smem + K2_LDS_K + wh * 4608 + (l < 3 ? l : 257 + l) * 16) = (u32x4){0u, 0u, 0u, 0u};
  if (wl == 2 && l < 2) *(u32x4*)(smem + KG_LDS_KC + wh * KG_KCB + l * 16) = (u32x4){0u, 0u, 0u, 0u};
  if (w == 1 && l < 2) *(u32x4*)(smem + K3_LDS_SF + l * 16) = (u32x4){0u, 0u, 0u, 0u};
  raw_barrier();
  const unsigned tag0 = epoch + 1u, tag1 = tag0 * 0x9E3779B1u + 0x7F4A7C15u;
  const float dt = rbf(__expf(kc_c ? bfhi(v_ldt) : bflo(v_ldt)));
  float lr2[2], fr[2];
  if (wh == 0) {
    float s[16];
#pragma unroll
    for (int r = 0; r < 8; ++r) {
      float a = bflo(xr8[r]) - piv[r], bb = bfhi(xr8[r]) - piv[r];
      s[r] = a + bb;
      s[8 + r] = a * a + bb * bb;
    }
    float t8[8], t4[4];
#pragma unroll
    for (int i = 0; i < 8; ++i) {
      auto sw = __builtin_amdgcn_permlane32_swap(__float_as_uint(s[i]), __float_as_uint(s[8 + i]), false, false);
      t8[i] = __uint_as_float(sw[0]) + __uint_as_float(sw[1]);
    }
#pragma unroll
    for (int i = 0; i < 4; ++i) {
      auto sw = __builtin_amdgcn_permlane16_swap(__float_as_uint(t8[i]), __float_as_uint(t8[4 + i]), false, false);
      t4[i] = row16_sum(__uint_as_float(sw[0]) + __uint_as_float(sw[1]));
    }
    if (l15 == 0) *(f32x4*)(red + wl * 16 + g * 4) = (f32x4){t4[0], t4[1], t4[2], t4[3]};
    asm volatile("s_waitcnt lgkmcnt(0)" ::: "memory");
    if (l == 0) lnflag[wl] = 1u;
    if (wl == 0) {
#pragma unroll 1
      for (unsigned it = 0; it < (1u << 20); ++it) {
        unsigned f = lnflag[l & 3];
        if (__all(f == 1u)) break;
        __builtin_amdgcn_s_sleep(1);
      }
      asm volatile("" ::: "memory");
      const int r = l & 7;
      float t1 = red[r] + red[16 + r] + red[32 + r] + red[48 + r];
      float t2 = red[8 + r] + red[24 + r] + red[40 + r] + red[56 + r];
      float pv = piv[0];
#pragma unroll
      for (int k = 1; k < 8; ++k) pv = r == k ? piv[k] : pv;
      float dm = t1 * (1.f / 512.f);
      float var = fmaxf(t2 * (1.f / 512.f) - dm * dm, 0.f);
      const unsigned md = pk_bf16(pv + dm, __builtin_amdgcn_sqrtf(rbf(rbf(var) + 1e-5f)));
      if (l < 8) store8_dev(ws + WS_G0 + (b * 8 + l) * 8, (u32x2){md, tag0});
    }
    STAMP(0, 1);
    {
      char* wlp = smem + P1_LDS_W + wl * 5120;
      const __amdgpu_buffer_rsrc_t rwt = __builtin_amdgcn_make_buffer_rsrc(ws + WS_W1T, 0, (NFTP * 16 + 32 * NKSP) * 1024, 0x00020000);
      if (wl == 2 && b < 32) __builtin_amdgcn_raw_buffer_store_b128((u32x4){0u, 0u, 0u, 0u}, rwt, (86 * 16 + b) * 1024 + l * 16, 0, 16);
#pragma unroll
      for (int it = 0; it < 2; ++it) {
        const int bi = gw + it * 1024;
        if (bi < 1376) {
          char* t = wlp + it * 2560;
          *(u32x4*)(t + (l >> 2) * 80 + (l & 3) * 16) = wv[it][0];
          *(u32x4*)(t + (16 + (l >> 2)) * 80 + (l & 3) * 16) = wv[it][1];
          const bool isw1 = bi < 688;
          const int q = l15 >> 2, p = l15 & 3;
          const unsigned a0 = lds_addr(t) + (isw1 ? (8 * g + q) : (4 * g + q)) * 80 + p * 8;
          const unsigned dr = isw1 ? 4 * 80 : 16 * 80;
#pragma unroll
          for (int ct = 0; ct < 2; ++ct) {
            u32x2 lo = tr_read(a0 + ct * 32), hi = tr_read(a0 + ct * 32 + dr);
            u32x4 o = {lo[0], lo[1], hi[0], hi[1]};
            unsigned off;
            if (isw1) { const int ks = bi / 43, fb = bi - ks * 43; off = ((2 * fb + ct) * 16 + ks) * 1024; }
            else { const int b2 = bi - 688, ks = b2 >> 4; off = (NFTP * 16 + (2 * (b2 & 15) + ct) * NKSP + ks) * 1024; }
            __builtin_amdgcn_raw_buffer_store_b128(o, rwt, off + l * 16, 0, 16);
          }
        }
      }
    }
    lr2[0] = (-rbf(__expf(bflo(v_lar))) * dt) * 1.4426950408889634f;
    lr2[1] = (-rbf(__expf(bfhi(v_lar))) * dt) * 1.4426950408889634f;
    fr[0] = (bflo(v_aim) * dt) * 0.15915494309189535f;
    fr[1] = (bfhi(v_aim) * dt) * 0.15915494309189535f;
    const float cm = a_chain(smem + KG_LDS_A + kc_c * 32 * KG_AROW + l * 8, 2 * part, lr2, fr, v_cr, v_ci);
    if (part == 0 && l == 0) red[64 + 8 + kc_c] = 2.f * dt * cm * 128.f;
    asm volatile("s_waitcnt vmcnt(0)" ::: "memory");
    STAMP(0, 2);
    raw_barrier();
    if (tid == 192) store8_dev(ws + WS_FW + b * 8, (u32x2){tag0, tag1});
    {
      const char* Ab = smem + KG_LDS_A + kc_c * 32 * KG_AROW + (16 * part + l15) * KG_AROW + g * 16;
      const char* B0 = smem + KG_LDS_B + kc_c * 21 * KG_AROW + l15 * KG_AROW + g * 16;
      const char* B1 = smem + KG_LDS_B + kc_c * 21 * KG_AROW + (16 + (l15 < 5 ? l15 : 4)) * KG_AROW + g * 16;
      f16x8 af[8], b0[8], b1f[8];
#pragma unroll
      for (int q = 0; q < 8; ++q) { af[q] = *(const f16x8*)(Ab + q * 64); b0[q] = *(const f16x8*)(B0 + q * 64); b1f[q] = *(const f16x8*)(B1 + q * 64); }
      f32x4 acc0 = {0, 0, 0, 0}, acc1 = {0, 0, 0, 0};
#pragma unroll
      for (int q = 0; q < 8; ++q) {
        acc0 = __builtin_amdgcn_mfma_f32_16x16x32_f16(af[q], b0[q], acc0, 0, 0, 0);
        acc1 = __builtin_amdgcn_mfma_f32_16x16x32_f16(af[q], b1f[q], acc1, 0, 0, 0);
      }
      _Float16* kc = (_Float16*)(smem + KG_LDS_KC + kc_c * KG_KCB + 32);
      const int a0 = 16 * part + 4 * g;
      int base, stride;
      if (l15 < 8) { base = l15; stride = 8; }
      else if (l15 < 12) { base = 256 + (l15 - 8); stride = 4; }
      else { base = 384 + 64 * ((l15 - 12) >> 1) + (l15 & 1); stride = 2; }
#pragma unroll
      for (int r = 0; r < 4; ++r) {
        kc[base + stride * (a0 + r)] = (_Float16)(acc0[r] * (1.f / 128.f));
        if (l15 < 4 || (l15 == 4 && a0 + r == 0)) kc[512 + 32 * l15 + a0 + r] = (_Float16)(acc1[r] * (1.f / 128.f));
      }
    }
    STAMP(0, 3);
    raw_barrier();
    u32x4 fwa = {0u, 0u, 0u, 0u}, fwb = {0u, 0u, 0u, 0u};
    if (w == 3) {
      fwa = __builtin_amdgcn_raw_buffer_load_b128(rsy, WS_FW + l * 32, 0, 16);
      fwb = __builtin_amdgcn_raw_buffer_load_b128(rsy, WS_FW + l * 32 + 16, 0, 16);
    }
    __builtin_amdgcn_sched_barrier(0);
    {
      const int c2 = tid + 1;
      int ih; unsigned tbl;
      if (c2 <= 32) { ih = 8 * c2; tbl = 0x76543210u; }
      else if (c2 <= 64) { ih = 256 + 4 * (c2 - 32); tbl = 0x43222100u; }
      else if (c2 <= 128) { ih = 384 + 2 * (c2 - 64); tbl = 0x22111000u; }
      else { ih = 512 + (c2 - 128); tbl = 0x11100000u | ((unsigned)(c2 & 1) << 16); }
#pragma unroll
      for (int c = 0; c < 2; ++c) {
        const u16* kc0 = (const u16*)(smem + KG_LDS_KC + c * KG_KCB + 32);
        const u16* kc = kc0 + ih;
        unsigned v[8];
#pragma unroll
        for (int j = 0; j < 8; ++j) v[j] = kc[-(int)((tbl >> (4 * j)) & 15u)];
        if (c2 == 256) v[0] = 0u;
        u32x4 o = {v[0] | (v[1] << 16), v[2] | (v[3] << 16), v[4] | (v[5] << 16), v[6] | (v[7] << 16)};
        *(u32x4*)(smem + K2_LDS_K + c * 4608 + (259 - c2) * 16) = o;
        if (c2 == 1) *(u32x4*)(smem + K2_LDS_K + c * 4608 + 259 * 16) = (u32x4){(unsigned)kc0[0], 0u, 0u, 0u};
      }
    }
    if (w == 3) {
#pragma unroll 1
      for (unsigned it = 0; it < SPIN_MAX; ++it) {
        if (__all(flags_ok(fwa, tag0, tag1) && flags_ok(fwb, tag0, tag1))) break;
        __builtin_amdgcn_s_sleep(2);
        fwa = __builtin_amdgcn_raw_buffer_load_b128(rsy, WS_FW + l * 32, 0, 16);
        fwb = __builtin_amdgcn_raw_buffer_load_b128(rsy, WS_FW + l * 32 + 16, 0, 16);
      }
    }
    STAMP(0, 5);
  } else {
    const int at = tid - 256;
    const __amdgpu_buffer_rsrc_t rxt = __builtin_amdgcn_make_buffer_rsrc(ws + WS_XT, 0, DM * XT_PITCH, 0x00020000);
    {
      char* tl = smem + K2_LDS_X + wl * 2048;
      *(u32x4*)(tl + l * 16) = xt[0];
      *(u32x4*)(tl + 1024 + l * 16) = xt[1];
      const int q = l15 >> 2, p = l15 & 3;
      const unsigned ta = lds_addr(tl) + (8 * g + q) * 32 + p * 8;
      u32x4 pc[2];
#pragma unroll
      for (int i = 0; i < 2; ++i) {
        u32x2 lo = tr_read(ta + i * 1024), hi = tr_read(ta + i * 1024 + 128);
        pc[i] = (u32x4){lo[0], lo[1], hi[0], hi[1]};
      }
      asm volatile("s_waitcnt lgkmcnt(0)" ::: "memory");
#pragma unroll
      for (int i = 0; i < 2; ++i) *(u32x4*)(tl + l15 * 128 + (g + 4 * i) * 16) = pc[i];
#pragma unroll
      for (int i = 0; i < 2; ++i) {
        const u32x4 o = *(const u32x4*)(tl + i * 1024 + l * 16);
        const int ch = (b & 7) * 64 + wl * 16 + i * 8 + (l >> 3);
        __builtin_amdgcn_raw_buffer_store_b128(o, rxt, ch * XT_PITCH + (b >> 3) * 128 + (l & 7) * 16, 0, 16);
      }
      asm volatile("s_waitcnt vmcnt(0)" ::: "memory");
      if (l == 0) lnflag[4 + wl] = 1u;
      if (wl == 0) {
#pragma unroll 1
        for (unsigned it = 0; it < (1u << 20); ++it) {
          unsigned f = lnflag[4 + (l & 3)];
          if (__all(f == 1u)) break;
          __builtin_amdgcn_s_sleep(1);
        }
        if (l == 0) store8_dev(ws + WS_FX + ((b & 7) * 32 + (b >> 3)) * 8, (u32x2){tag0, tag1});
      }
    }
    STAMP(0, 4);
    lr2[0] = (-rbf(__expf(bflo(v_lar))) * dt) * 1.4426950408889634f;
    lr2[1] = (-rbf(__expf(bfhi(v_lar))) * dt) * 1.4426950408889634f;
    fr[0] = (bflo(v_aim) * dt) * 0.15915494309189535f;
    fr[1] = (bfhi(v_aim) * dt) * 0.15915494309189535f;
    {
      char* Bimg = smem + KG_LDS_B + kc_c * 21 * KG_AROW + l * 8;
#define PUTB(col, v0, v1) { cpx _a = (v0), _b = (v1); u32x2 _o = {pk_f16(_a.r, -_a.i), pk_f16(_b.r, -_b.i)}; *(u32x2*)(Bimg + (col) * KG_AROW) = _o; }
      if (part == 0) {
        cpx z[2][8], e256[2];
#pragma unroll
        for (int m = 0; m < 2; ++m) {
          z[m][0].r = 1.f; z[m][0].i = 0.f;
          z[m][1] = cexp_dir(lr2[m], fr[m], 1.f);
          z[m][2] = cmul(z[m][1], z[m][1]); z[m][3] = cmul(z[m][2], z[m][1]); z[m][4] = cmul(z[m][2], z[m][2]);
          z[m][5] = cmul(z[m][4], z[m][1]); z[m][6] = cmul(z[m][4], z[m][2]); z[m][7] = cmul(z[m][4], z[m][3]);
          e256[m] = cexp_dir(lr2[m], fr[m], 256.f);
        }
#pragma unroll
        for (int k = 0; k < 8; ++k) PUTB(k, z[0][k], z[1][k]);
        PUTB(8, e256[0], e256[1]);
#pragma unroll
        for (int k = 1; k < 4; ++k) PUTB(8 + k, cmul(e256[0], z[0][2 * k]), cmul(e256[1], z[1][2 * k]));
      } else {
        cpx z4[2], e256[2], e512[2], e768[2], e1024[2];
#pragma unroll
        for (int m = 0; m < 2; ++m) {
          z4[m] = cexp_dir(lr2[m], fr[m], 4.f);
          e256[m] = cexp_dir(lr2[m], fr[m], 256.f);
          e1024[m] = cexp_dir(lr2[m], fr[m], 1024.f);
          e512[m] = cmul(e256[m], e256[m]); e768[m] = cmul(e512[m], e256[m]);
        }
        PUTB(12, e512[0], e512[1]); PUTB(13, cmul(e512[0], z4[0]), cmul(e512[1], z4[1]));
        PUTB(14, e768[0], e768[1]); PUTB(15, cmul(e768[0], z4[0]), cmul(e768[1], z4[1]));
        PUTB(16, e1024[0], e1024[1]);
        PUTB(17, cmul(e1024[0], e256[0]), cmul(e1024[1], e256[1]));
        PUTB(18, cmul(e1024[0], e512[0]), cmul(e1024[1], e512[1]));
        PUTB(19, cmul(e1024[0], e768[0]), cmul(e1024[1], e768[1]));
        PUTB(20, cmul(e1024[0], e1024[0]), cmul(e1024[1], e1024[1]));
      }
#undef PUTB
    }
    a_chain(smem + KG_LDS_A + kc_c * 32 * KG_AROW + l * 8, 2 * part + 1, lr2, fr, v_cr, v_ci);
    STAMP(0, 1);
    raw_barrier();
    STAMP(0, 2);
    const int p0 = 8 * at;
    const unsigned upos = ((p0 >> 7) + 2) * K2_ROWB + (p0 & 127) * 2;
    const bool dup = (p0 & 127) == 0;
    u32x4 uo[2];
    {
#pragma unroll 1
      for (unsigned it = 0; it < SPIN_MAX; ++it) {
        u32x4 fa = __builtin_amdgcn_raw_buffer_load_b128(rsy, WS_FX + (cp >> 5) * 256 + (l & 15) * 16, 0, 16);
        if (__all(flags_ok(fa, tag0, tag1))) break;
        __builtin_amdgcn_s_sleep(1);
      }
      asm volatile("" ::: "memory");
      u32x4 xq[2];
#pragma unroll
      for (int c = 0; c < 2; ++c) xq[c] = __builtin_amdgcn_raw_buffer_load_b128(rxt, (2 * cp + c) * XT_PITCH + at * 16, 0, 16);
      u32x4 gq[4];
#pragma unroll 1
      for (unsigned it = 0; it < SPIN_MAX; ++it) {
        bool ok = true;
#pragma unroll
        for (int k = 0; k < 4; ++k) {
          gq[k] = __builtin_amdgcn_raw_buffer_load_b128(rsy, WS_G0 + at * 64 + k * 16, 0, 16);
          ok = ok && gq[k][1] == tag0 && gq[k][3] == tag0;
        }
        if (__all(ok)) break;
        __builtin_amdgcn_s_sleep(1);
      }
#pragma unroll
      for (int k = 0; k < 4; ++k) {
        const float m0 = bflo(gq[k][0]), r0 = __builtin_amdgcn_rcpf(bfhi(gq[k][0]));
        const float m1 = bflo(gq[k][2]), r1 = __builtin_amdgcn_rcpf(bfhi(gq[k][2]));
#pragma unroll
        for (int c = 0; c < 2; ++c) {
          unsigned dd = pk_bf16(bflo(xq[c][k]) - m0, bfhi(xq[c][k]) - m1);
          unsigned uu = pk_bf16(bflo(dd) * r0, bfhi(dd) * r1);
          uo[c][k] = pk_f16(bflo(uu), bfhi(uu));
        }
      }
#pragma unroll
      for (int c = 0; c < 2; ++c) {
        *(u32x4*)(smem + K2_LDS_X + c * 4096 + p0 * 2) = xq[c];
        *(u32x4*)(smem + K2_LDS_U + c * K2_UCH + upos) = uo[c];
        if (dup) *(u32x4*)(smem + K2_LDS_U + c * K2_UCH + upos - K2_ROWB + 256) = uo[c];
      }
    }
    STAMP(0, 3);
    raw_barrier();
#pragma unroll
    for (int c = 0; c < 2; ++c) {
      const u32x2 nx = *(const u32x2*)(smem + K2_LDS_U + c * K2_UCH + upos + 16);
      const unsigned d[6] = {uo[c][0], uo[c][1], uo[c][2], uo[c][3], nx[0], nx[1]};
      unsigned e[5];
#pragma unroll
      for (int i = 0; i < 5; ++i) e[i] = __builtin_amdgcn_alignbit(d[i + 1], d[i], 16);
      const u32x4 c1 = {e[0], e[1], e[2], e[3]}, c2 = {d[1], d[2], d[3], d[4]}, c3 = {e[1], e[2], e[3], e[4]};
      char* dst = smem + K2_LDS_U + c * K2_UCH + upos;
      *(u32x4*)(dst + 1 * K2_COPY) = c1;
      *(u32x4*)(dst + 2 * K2_COPY) = c2;
      *(u32x4*)(dst + 3 * K2_COPY) = c3;
      if (dup) {
        *(u32x4*)(dst + 1 * K2_COPY - K2_ROWB + 256) = c1;
        *(u32x4*)(dst + 2 * K2_COPY - K2_ROWB + 256) = c2;
        *(u32x4*)(dst + 3 * K2_COPY - K2_ROWB + 256) = c3;
      }
      if (at == 0) {
        char* dn = smem + K2_LDS_U + c * K2_UCH + K2_ROWB + 124 * 2;
        *(u32x2*)(dn + 1 * K2_COPY) = (u32x2){0u, d[0] << 16};
        *(u32x2*)(dn + 2 * K2_COPY) = (u32x2){0u, d[0]};
        *(u32x2*)(dn + 3 * K2_COPY) = (u32x2){d[0] << 16, e[0]};
      }
    }
    STAMP(0, 5);
  }
  raw_barrier();
  const float ksc = red[64 + 8 + wh];
  const int myft = cg * 11 + 3 * wl;
  bf16x8 wf[3][8];
  const __amdgpu_buffer_rsrc_t rw1 = __builtin_amdgcn_make_buffer_rsrc(ws + WS_W1T, 0, NFTP * 16 * 1024, 0x00020000);
  const unsigned w1o = (myft * 16 + wh * 8) * 1024;
#define LDW1(a, k) __builtin_bit_cast(bf16x8, __builtin_amdgcn_raw_buffer_load_b128(rw1, l * 16, w1o + (a) * 16 * 1024 + (k) * 1024, 16))
  STAMP(1, 1);
  const u32x2 b2v = *(const u32x2*)(b2 + cg * 64 + (w >> 1) * 16 + 4 * g);
  u32x2 b1v[3];
#pragma unroll
  for (int a = 0; a < 3; ++a) {
    const int f = 16 * (myft + a) + 4 * g;
    b1v[a] = *(const u32x2*)(b1 + (f < FF ? f : 0));
  }
  {
    f32x4 acc[8];
#pragma unroll
    for (int i = 0; i < 8; ++i) acc[i] = (f32x4){0, 0, 0, 0};
    const int phi = l15 & 7, nu = l15 >> 3;
    const char* Fb = smem + K2_LDS_K + wh * 4608 + (259 - l15 + g) * 16 + 192 - 64 * wl;
    const char* Gb = smem + K2_LDS_U + wh * K2_UCH + (phi & 3) * K2_COPY + (nu + 2) * K2_ROWB + ((phi & 4) + 8 * g) * 2 + 192 - 64 * wl;
#pragma unroll
    for (int rr = 0; rr < 2; ++rr) {
      f16x8 F[8], G[8];
#pragma unroll
      for (int q = 0; q < 8; ++q) { G[q] = ld_g(Gb - rr * K2_ROWB + q * 2 * K2_ROWB); F[q] = *(const f16x8*)(Fb - rr * 256 - q * 512); }
      __builtin_amdgcn_sched_barrier(0);
#pragma unroll
      for (int k = 0; k < 8; ++k) wf[rr][k] = LDW1(rr, k);
#pragma unroll
      for (int e = 0; e < 8; ++e)
#pragma unroll
        for (int q = 0; q + e < 8; ++q)
          acc[q + e] = __builtin_amdgcn_mfma_f32_16x16x32_f16(G[e], F[q], acc[q + e], 0, 0, 0);
#pragma unroll
      for (int k = 0; k < 8; ++k) {
        __builtin_amdgcn_sched_group_barrier(0x008, 4, 0);
        __builtin_amdgcn_sched_group_barrier(0x020, 1, 0);
      }
      __builtin_amdgcn_sched_barrier(0);
      if (rr == 0 && wl == 0) {
        f16x8 Gm = ld_g(Gb - 2 * K2_ROWB), F8 = *(const f16x8*)(Fb - 8 * 512);
#pragma unroll
        for (int i = 0; i < 7; ++i) acc[i] = __builtin_amdgcn_mfma_f32_16x16x32_f16(Gm, F[i + 1], acc[i], 0, 0, 0);
        acc[7] = __builtin_amdgcn_mfma_f32_16x16x32_f16(Gm, F8, acc[7], 0, 0, 0);
      }
    }
    STAMP(1, 2);
#pragma unroll
    for (int i = 0; i < 8; ++i) *(f32x4*)(smem + K2_LDS_Y + (w * 8 + i) * 1024 + l * 16) = acc[i];
    raw_barrier();
    const float dk = wh ? bfhi(dkv) : bflo(dkv);
    const int tl = 128 * (g >> 1) + 8 * l15 + 4 * (g & 1);
#pragma unroll
    for (int i = 0; i < 2; ++i) {
      const int ti = 2 * wl + i;
      f32x4 o = *(const f32x4*)(smem + K2_LDS_Y + ((wh * 4 + 0) * 8 + ti) * 1024 + l * 16);
#pragma unroll
      for (int k = 1; k < 4; ++k) o += *(const f32x4*)(smem + K2_LDS_Y + ((wh * 4 + k) * 8 + ti) * 1024 + l * 16);
      const int t0 = 256 * ti + tl;
      u32x2 uu = *(const u32x2*)(smem + K2_LDS_U + wh * K2_UCH + ((t0 >> 7) + 2) * K2_ROWB + (t0 & 127) * 2);
      u32x2 xx = *(const u32x2*)(smem + K2_LDS_X + wh * 4096 + t0 * 2);
      u32x2 res;
#pragma unroll
      for (int j = 0; j < 2; ++j) {
        unsigned yy = pk_bf16(o[2 * j] * ksc, o[2 * j + 1] * ksc);
        unsigned ud = pk_bf16(h2f_lo(uu[j]) * dk, h2f_hi(uu[j]) * dk);
        unsigned z = pk_bf16(bflo(yy) + bflo(ud), bfhi(yy) + bfhi(ud));
        res[j] = pk_bf16(bflo(xx[j]) + bflo(z), bfhi(xx[j]) + bfhi(z));
      }
      store8_dev(ws + WS_HT + (size_t)(2 * cp + wh) * XT_PITCH + t0 * 2, res);
    }
  }
  STAMP(1, 3);
  asm volatile("s_waitcnt vmcnt(0)" ::: "memory");
  raw_barrier();
  if (tid == 192) store8_dev(ws + WS_F1 + b * 8, (u32x2){tag0, tag1});
  STAMP(1, 4);
  const int ks0 = w * 5 + (w < 3 ? w : 3);
  bf16x8 w2f[6][4];
  const __amdgpu_buffer_rsrc_t rw2 = __builtin_amdgcn_make_buffer_rsrc(ws + WS_W2T, 0, 32 * NKSP * 1024, 0x00020000);
  const unsigned w2o = ((cg * 4) * NKSP + ks0) * 1024;
#define LDW2(k, a) __builtin_bit_cast(bf16x8, __builtin_amdgcn_raw_buffer_load_b128(rw2, l * 16, w2o + ((a) * NKSP + (k)) * 1024, 16))
  if (wl < 3) {
#pragma unroll
    for (int k = 0; k < 8; ++k) wf[2][k] = LDW1(2, k);
  } else {
#pragma unroll
    for (int k = 0; k < 8; ++k) wf[2][k] = wf[1][k];
  }
  __builtin_amdgcn_sched_barrier(0);
  const u32x4 onesu = {0x3f803f80u, 0x3f803f80u, 0x3f803f80u, 0x3f803f80u};
  const bf16x8 onesf = __builtin_bit_cast(bf16x8, onesu);
  {
    f32x4 cq[2];
#pragma unroll
    for (int a = 0; a < 2; ++a) cq[a] = (f32x4){0, 0, 0, 0};
#pragma unroll
    for (int k = 0; k < 8; ++k)
#pragma unroll
      for (int a = 0; a < 2; ++a) cq[a] = __builtin_amdgcn_mfma_f32_16x16x32_bf16(wf[a][k], onesf, cq[a], 0, 0, 0);
#pragma unroll
    for (int a = 0; a < 2; ++a) *(f32x4*)(smem + K3_LDS_E + (w * 9 + 6 + a) * 1024 + l * 16) = cq[a];
  }
  STAMP(1, 5);
  if (w == 3) {
#pragma unroll 1
    for (unsigned it = 0; it < SPIN_MAX; ++it) {
      u32x4 fa = __builtin_amdgcn_raw_buffer_load_b128(rsy, WS_F1 + l * 32, 0, 16);
      u32x4 fb = __builtin_amdgcn_raw_buffer_load_b128(rsy, WS_F1 + l * 32 + 16, 0, 16);
      if (__all(flags_ok(fa, tag0, tag1) && flags_ok(fb, tag0, tag1))) break;
      __builtin_amdgcn_s_sleep(1);
    }
    if (b == 0 && l == 0) __hip_atomic_store((unsigned*)(ws + WS_EPOCH), tag0, __ATOMIC_RELAXED, __HIP_MEMORY_SCOPE_AGENT);
  }
  raw_barrier();
  STAMP(2, 0);
  asm volatile("" : "+v"(wf[2][0]), "+v"(wf[2][1]), "+v"(wf[2][2]), "+v"(wf[2][3]), "+v"(wf[2][4]), "+v"(wf[2][5]), "+v"(wf[2][6]), "+v"(wf[2][7]) : "v"(b2v), "v"(b1v[0]), "v"(b1v[1]), "v"(b1v[2]) : "memory");
#pragma unroll
  for (int it = 0; it < 8; ++it) {
    const int j = it * 8 + w;
    const int row = j * 8 + (l >> 3), pc = l & 7;
    const int swz = ((row >> 1) & 1) | (((row >> 3) & 1) << 1);
    const int lc = (((pc >> 1) ^ swz) << 1) | (pc & 1);
    glds16_sc1(ws + WS_HT + (size_t)row * XT_PITCH + tb * 128 + lc * 16, sb + K3_LDS_H + j * 1024);
  }
  {
    f32x4 cq = {0, 0, 0, 0};
#pragma unroll
    for (int k = 0; k < 8; ++k) cq = __builtin_amdgcn_mfma_f32_16x16x32_bf16(wf[2][k], onesf, cq, 0, 0, 0);
    *(f32x4*)(smem + K3_LDS_E + (w * 9 + 8) * 1024 + l * 16) = cq;
  }
  asm volatile("s_waitcnt vmcnt(0)" ::: "memory");
  raw_barrier();
  STAMP(2, 1);
  const int q4 = l15 >> 2, p4 = l15 & 3;
  const __amdgpu_buffer_rsrc_t rs = __builtin_amdgcn_make_buffer_rsrc(ws + WS_HACT, 0, 64 * NFTP * 1024, 0x00020000);
  u32x2 hres[2];
  {
    const int swz = ((q4 >> 1) & 1) | ((g & 1) << 1);
    const unsigned lbase = sb + K3_LDS_H + wh * 8 * 4096 + (8 * g + q4) * 128 + p4 * 8;
    unsigned ad[4];
#pragma unroll
    for (int t = 0; t < 4; ++t) ad[t] = lbase + (((t ^ (2 * wh)) ^ swz) << 5);
    const unsigned sa = lbase + ((wl ^ swz) << 5);
    const unsigned vo2 = wl < 3 ? (unsigned)(l * 16) : 0x7f000000u;
    f32x4 acc[3][2];
    bf16x8 bf[2][2];
    auto finish = [&](int p) {
      f32x4 cs[3], bb[3], co[3], pa[3][2];
      f32x2 st[2];
#pragma unroll
      for (int t = 0; t < 2; ++t) st[t] = *(const f32x2*)(smem + K3_LDS_ST + ((2 * p + t) * 16 + l15) * 8);
#pragma unroll
      for (int a = 0; a < 3; ++a) {
        cs[a] = *(const f32x4*)(smem + K3_LDS_E + (w * 9 + 6 + a) * 1024 + l * 16);
        co[a] = *(const f32x4*)(smem + K3_LDS_E + ((w ^ 4) * 9 + 6 + a) * 1024 + l * 16);
#pragma unroll
        for (int t = 0; t < 2; ++t) pa[a][t] = *(const f32x4*)(smem + K3_LDS_E + ((w ^ 4) * 9 + a * 2 + t) * 1024 + l * 16);
      }
      __builtin_amdgcn_sched_barrier(0);
#pragma unroll
      for (int a = 0; a < 3; ++a) {
        bb[a] = (f32x4){bflo(b1v[a][0]), bfhi(b1v[a][0]), bflo(b1v[a][1]), bfhi(b1v[a][1])};
        cs[a] += co[a];
#pragma unroll
        for (int t = 0; t < 2; ++t) acc[a][t] += pa[a][t];
      }
      const unsigned so = ((tb * 2 + p) * NFTP + myft) * 1024;
      const f32x2 c1 = {-0.10294324f, -0.10294324f}, c0 = {-2.30220819f, -2.30220819f}, one = {1.f, 1.f};
      f32x2 v[12], q[12];
#pragma unroll
      for (int i = 0; i < 12; ++i) {
        const int a = i >> 2, t = (i >> 1) & 1, r = 2 * (i & 1);
        const f32x2 a2 = {st[t][0], st[t][0]}, b2 = {st[t][1], st[t][1]};
        v[i] = __builtin_elementwise_fma(a2, (f32x2){acc[a][t][r], acc[a][t][r + 1]},
                                         __builtin_elementwise_fma(b2, (f32x2){cs[a][r], cs[a][r + 1]}, (f32x2){bb[a][r], bb[a][r + 1]}));
      }
      __builtin_amdgcn_sched_barrier(0);
#pragma unroll
      for (int i = 0; i < 12; ++i) q[i] = v[i] * v[i];
      __builtin_amdgcn_sched_barrier(0);
#pragma unroll
      for (int i = 0; i < 12; ++i) q[i] = __builtin_elementwise_fma(q[i], c1, c0);
      __builtin_amdgcn_sched_barrier(0);
#pragma unroll
      for (int i = 0; i < 12; ++i) q[i] = q[i] * v[i];
      __builtin_amdgcn_sched_barrier(0);
#pragma unroll
      for (int i = 0; i < 12; ++i) q[i] = (f32x2){__builtin_amdgcn_exp2f(q[i][0]), __builtin_amdgcn_exp2f(q[i][1])};
      __builtin_amdgcn_sched_barrier(0);
#pragma unroll
      for (int i = 0; i < 12; ++i) q[i] = q[i] + one;
      __builtin_amdgcn_sched_barrier(0);
#pragma unroll
      for (int i = 0; i < 12; ++i) q[i] = (f32x2){__builtin_amdgcn_rcpf(q[i][0]), __builtin_amdgcn_rcpf(q[i][1])};
      __builtin_amdgcn_sched_barrier(0);
#pragma unroll
      for (int i = 0; i < 12; ++i) v[i] = v[i] * q[i];
      __builtin_amdgcn_sched_barrier(0);
#pragma unroll
      for (int a = 0; a < 3; ++a) {
        u32x4 o = {pk_bf16(v[4 * a][0], v[4 * a][1]), pk_bf16(v[4 * a + 1][0], v[4 * a + 1][1]),
                   pk_bf16(v[4 * a + 2][0], v[4 * a + 2][1]), pk_bf16(v[4 * a + 3][0], v[4 * a + 3][1])};
        __builtin_amdgcn_raw_buffer_store_b128(o, rs, (a < 2 ? (unsigned)(l * 16) : vo2) + so + a * 1024, 0, 16);
      }
      __builtin_amdgcn_sched_barrier(0);
    };
    volatile LDS_AS unsigned* sflag = (volatile LDS_AS unsigned*)(size_t)(sb + K3_LDS_SF);
    {
      if (wh == 0) {
        f32x4 sS = {0, 0, 0, 0}, sQ = {0, 0, 0, 0};
        bf16x8 sf[16];
#pragma unroll
        for (int ks = 0; ks < 16; ++ks) sf[ks] = ld_bfrag(sa + ks * 4096);
#pragma unroll
        for (int ks = 0; ks < 16; ++ks) {
          sS = __builtin_amdgcn_mfma_f32_16x16x32_bf16(onesf, sf[ks], sS, 0, 0, 0);
          sQ = __builtin_amdgcn_mfma_f32_16x16x32_bf16(sf[ks], sf[ks], sQ, 0, 0, 0);
        }
        float dq = (l15 & 2) ? ((l15 & 1) ? sQ[3] : sQ[2]) : ((l15 & 1) ? sQ[1] : sQ[0]);
        float s2 = __shfl(dq, l15 + 16 * (l15 >> 2));
        float mean = sS[0] * (1.f / 512.f);
        float var = fmaxf(s2 * (1.f / 512.f) - mean * mean, 0.f);
        float m1 = rbf(mean);
        float r1 = __builtin_amdgcn_rcpf(rbf(__builtin_amdgcn_sqrtf(rbf(rbf(var) + 1e-5f))));
        float mean2 = (mean - m1) * r1, var2 = var * r1 * r1;
        float m2 = rbf(mean2);
        float r2 = __builtin_amdgcn_rcpf(rbf(__builtin_amdgcn_sqrtf(rbf(rbf(var2) + 1e-5f))));
        if (l < 16) *(f32x2*)(smem + K3_LDS_ST + (wl * 16 + l) * 8) = (f32x2){r1 * r2, -(m1 * r1 + m2) * r2};
      }
#pragma unroll
      for (int a = 0; a < 3; ++a) { acc[a][0] = (f32x4){0, 0, 0, 0}; acc[a][1] = (f32x4){0, 0, 0, 0}; }
      bf[0][0] = ld_bfrag(ad[2]); bf[0][1] = ld_bfrag(ad[3]);
      __builtin_amdgcn_sched_barrier(0);
#pragma unroll
      for (int ks = 0; ks < 8; ++ks) {
        const int cur = ks & 1, nx = cur ^ 1;
        if (ks < 7) {
          bf[nx][0] = ld_bfrag(ad[2] + (ks + 1) * 4096); bf[nx][1] = ld_bfrag(ad[3] + (ks + 1) * 4096);
        } else {
          bf[nx][0] = ld_bfrag(ad[0]); bf[nx][1] = ld_bfrag(ad[1]);
        }
        w2f[ks >> 2][ks & 3] = LDW2(ks >> 2, ks & 3);
#pragma unroll
        for (int t = 0; t < 2; ++t)
#pragma unroll
          for (int a = 0; a < 3; ++a) acc[a][t] = __builtin_amdgcn_mfma_f32_16x16x32_bf16(wf[a][ks], bf[cur][t], acc[a][t], 0, 0, 0);
#pragma unroll
        for (int k = 0; k < 4; ++k) {
          __builtin_amdgcn_sched_group_barrier(0x008, 1, 0);
          __builtin_amdgcn_sched_group_barrier(0x100, 1, 0);
          if (k == 0) __builtin_amdgcn_sched_group_barrier(0x020, 1, 0);
        }
        __builtin_amdgcn_sched_barrier(0);
      }
#pragma unroll
      for (int a = 0; a < 3; ++a)
#pragma unroll
        for (int t = 0; t < 2; ++t) *(f32x4*)(smem + K3_LDS_E + (w * 9 + a * 2 + t) * 1024 + l * 16) = acc[a][t];
      asm volatile("s_waitcnt lgkmcnt(0)" ::: "memory");
      if (l == 0) sflag[w] = 1u;
    }
    STAMP(2, 2);
    {
#pragma unroll
      for (int a = 0; a < 3; ++a) { acc[a][0] = (f32x4){0, 0, 0, 0}; acc[a][1] = (f32x4){0, 0, 0, 0}; }
      __builtin_amdgcn_sched_barrier(0);
#pragma unroll
      for (int ks = 0; ks < 8; ++ks) {
        const int cur = ks & 1, nx = cur ^ 1;
        if (ks < 7) { bf[nx][0] = ld_bfrag(ad[0] + (ks + 1) * 4096); bf[nx][1] = ld_bfrag(ad[1] + (ks + 1) * 4096); }
        w2f[2 + (ks >> 2)][ks & 3] = LDW2(2 + (ks >> 2), ks & 3);
#pragma unroll
        for (int t = 0; t < 2; ++t)
#pragma unroll
          for (int a = 0; a < 3; ++a) acc[a][t] = __builtin_amdgcn_mfma_f32_16x16x32_bf16(wf[a][ks], bf[cur][t], acc[a][t], 0, 0, 0);
#pragma unroll
        for (int k = 0; k < 4; ++k) {
          __builtin_amdgcn_sched_group_barrier(0x008, 1, 0);
          if (ks < 7) __builtin_amdgcn_sched_group_barrier(0x100, 1, 0);
          if (k == 0) __builtin_amdgcn_sched_group_barrier(0x020, 1, 0);
        }
        __builtin_amdgcn_sched_barrier(0);
      }
    }
#pragma unroll
    for (int a = 0; a < 4; ++a) w2f[4][a] = LDW2(4, a);
    if (w < 3) {
#pragma unroll
      for (int a = 0; a < 4; ++a) w2f[5][a] = LDW2(5, a);
    }
    {
      const int row = cg * 64 + (w >> 1) * 16 + 4 * g + q4;
      const int swz2 = ((row >> 1) & 1) | (((row >> 3) & 1) << 1);
      const unsigned hb = sb + K3_LDS_H + row * 128 + p4 * 8;
#pragma unroll
      for (int i = 0; i < 2; ++i) hres[i] = tr_read(hb + (((2 * i + (w & 1)) ^ swz2) << 5));
    }
    STAMP(2, 6);
#pragma unroll 1
    for (unsigned it = 0; it < (1u << 20); ++it) {
      unsigned f = sflag[l & 7];
      if (__all(f == 1u)) break;
      __builtin_amdgcn_s_sleep(2);
    }
    asm volatile("" ::: "memory");
    STAMP(2, 3);
    finish(wh);
  }
  STAMP(2, 4);
  asm volatile("s_waitcnt vmcnt(0)" ::: "memory");
  raw_barrier();
  STAMP(2, 5);
  if (w == 0) {
    if (l == 0) store8_dev(ws + WS_F2 + tb * 128 + cg * 8, (u32x2){tag0, tag1});
#pragma unroll 1
    for (unsigned it = 0; it < SPIN_MAX; ++it) {
      u32x4 fa = __builtin_amdgcn_raw_buffer_load_b128(rsy, WS_F2 + tb * 128 + (l & 3) * 16, 0, 16);
      if (__all(flags_ok(fa, tag0, tag1))) break;
      __builtin_amdgcn_s_sleep(1);
    }
  }
  raw_barrier();
  STAMP(3, 0);
  {
    const unsigned hvo = ((tb * 2) * NFTP + ks0 * 2) * 1024;
    u32x4 hv[2][6][2];
#pragma unroll
    for (int p = 0; p < 2; ++p) {
#pragma unroll
      for (int k = 0; k < 5; ++k)
#pragma unroll
        for (int f = 0; f < 2; ++f)
          hv[p][k][f] = __builtin_amdgcn_raw_buffer_load_b128(rs, l * 16, hvo + (p * NFTP + 2 * k + f) * 1024, 16);
    }
    if (w < 3) {
#pragma unroll
      for (int p = 0; p < 2; ++p)
#pragma unroll
        for (int f = 0; f < 2; ++f)
          hv[p][5][f] = __builtin_amdgcn_raw_buffer_load_b128(rs, l * 16, hvo + (p * NFTP + 10 + f) * 1024, 16);
    }
    __builtin_amdgcn_sched_barrier(0);
#pragma unroll
    for (int p = 0; p < 2; ++p) {
      f32x4 acc[4][2];
#pragma unroll
      for (int a = 0; a < 4; ++a) { acc[a][0] = (f32x4){0, 0, 0, 0}; acc[a][1] = (f32x4){0, 0, 0, 0}; }
#pragma unroll
      for (int k = 0; k < 6; ++k) {
        if (k < 5 || w < 3) {
          u32x4 x0 = {hv[p][k][0][0], hv[p][k][0][1], hv[p][k][1][0], hv[p][k][1][1]};
          u32x4 x1 = {hv[p][k][0][2], hv[p][k][0][3], hv[p][k][1][2], hv[p][k][1][3]};
          bf16x8 h0 = __builtin_bit_cast(bf16x8, x0), h1 = __builtin_bit_cast(bf16x8, x1);
#pragma unroll
          for (int a = 0; a < 4; ++a) {
            acc[a][0] = __builtin_amdgcn_mfma_f32_16x16x32_bf16(w2f[k][a], h0, acc[a][0], 0, 0, 0);
            acc[a][1] = __builtin_amdgcn_mfma_f32_16x16x32_bf16(w2f[k][a], h1, acc[a][1], 0, 0, 0);
          }
        }
      }
      __builtin_amdgcn_sched_barrier(0);
#pragma unroll
      for (int a = 0; a < 4; ++a)
#pragma unroll
        for (int t = 0; t < 2; ++t) *(f32x4*)(smem + K4_LDS_R + ((w * 4 + a) * 4 + 2 * p + t) * 1024 + l * 16) = acc[a][t];
      __builtin_amdgcn_sched_barrier(0);
    }
    STAMP(3, 1);
    raw_barrier();
    STAMP(3, 2);
    const float bq[4] = {bflo(b2v[0]), bfhi(b2v[0]), bflo(b2v[1]), bfhi(b2v[1])};
#pragma unroll
    for (int i = 0; i < 2; ++i) {
      const int t = 2 * i + (w & 1);
      f32x4 sacc = *(const f32x4*)(smem + K4_LDS_R + ((0 * 4 + (w >> 1)) * 4 + t) * 1024 + l * 16);
#pragma unroll
      for (int ww = 1; ww < 8; ++ww) sacc += *(const f32x4*)(smem + K4_LDS_R + ((ww * 4 + (w >> 1)) * 4 + t) * 1024 + l * 16);
      float o[4];
      o[0] = bflo(hres[i][0]) + rbf(rbf(sacc[0]) + bq[0]);
      o[1] = bfhi(hres[i][0]) + rbf(rbf(sacc[1]) + bq[1]);
      o[2] = bflo(hres[i][1]) + rbf(rbf(sacc[2]) + bq[2]);
      o[3] = bfhi(hres[i][1]) + rbf(rbf(sacc[3]) + bq[3]);
      u32x2 pk = {pk_bf16(o[0], o[1]), pk_bf16(o[2], o[3])};
      *(u32x2*)(out + (size_t)(tb * 64 + t * 16 + l15) * DM + cg * 64 + (w >> 1) * 16 + 4 * g) = pk;
    }
  }
  STAMP(3, 3);
  STAMP_END(3, 4);
}

extern "C" void kernel_launch(void* const* d_in, const int* in_sizes, int n_in,
                              void* d_out, int out_size, void* d_ws, size_t ws_size,
                              hipStream_t stream) {
  fused_main<<<256, 512, 0, stream>>>((const u16*)d_in[0], (const u16*)d_in[1], (const u16*)d_in[2], (const u16*)d_in[3],
                                      (const u16*)d_in[4], (const u16*)d_in[5], (const u16*)d_in[6], (const u16*)d_in[8],
                                      (const u16*)d_in[9], (const u16*)d_in[10], (const u16*)d_in[11], (char*)d_ws, (u16*)d_out);
}
```

```cpp
#include <hip/hip_runtime.h>
#include <hip/hip_bf16.h>
#include <stdint.h>

typedef unsigned short u16;
typedef __attribute__((ext_vector_type(8))) _Float16 f16x8;
typedef __attribute__((ext_vector_type(2))) _Float16 f16x2;
typedef __attribute__((ext_vector_type(8))) __bf16 bf16x8;
typedef __attribute__((ext_vector_type(2))) __bf16 bf16x2;
typedef __attribute__((ext_vector_type(2))) float f32x2;
typedef __attribute__((ext_vector_type(4))) float f32x4;
typedef __attribute__((ext_vector_type(4))) unsigned u32x4;
typedef __attribute__((ext_vector_type(2))) unsigned u32x2;
typedef __attribute__((ext_vector_type(4))) short s16x4;
#define LDS_AS __attribute__((address_space(3)))

#define SEQ 2048
#define DM 512
#define NM 128
#define FF 1376
#define NFT 86
#define NKS2 43

#define NFTP 88
#define NKSP 44
#define XT_PITCH 4224
#define WS_EPOCH 0
#define WS_FW 1024
#define WS_F1 4096
#define WS_F2 8192
#define WS_FX 12288
#define WS_G0 16384
#define WS_W1T 32768
#define WS_W2T (WS_W1T + NFTP * 16 * 1024)
#define WS_XT (WS_W2T + 32 * NKSP * 1024)
#define WS_HT (WS_XT + DM * XT_PITCH)
#define WS_HACT (WS_HT + DM * XT_PITCH)
#define WS_END (WS_HACT + 64 * NFTP * 1024)


__device__ __forceinline__ float bf2f(unsigned v) { return __uint_as_float(v << 16); }
__device__ __forceinline__ float bflo(unsigned v) { return __uint_as_float(v << 16); }
__device__ __forceinline__ float bfhi(unsigned v) { return __uint_as_float(v & 0xffff0000u); }
__device__ __forceinline__ unsigned pk_bf16(float a, float b) {
  f32x2 v = {a, b};
  bf16x2 r = __builtin_convertvector(v, bf16x2);
  return __builtin_bit_cast(unsigned, r);
}
__device__ __forceinline__ unsigned pk_f16(float a, float b) {
  f32x2 v = {a, b};
  f16x2 r = __builtin_convertvector(v, f16x2);
  return __builtin_bit_cast(unsigned, r);
}
__device__ __forceinline__ float rbf(float a) { return bflo(pk_bf16(a, a)); }
__device__ __forceinline__ float h2f_lo(unsigned v) { f16x2 h = __builtin_bit_cast(f16x2, v); return (float)h[0]; }
__device__ __forceinline__ float h2f_hi(unsigned v) { f16x2 h = __builtin_bit_cast(f16x2, v); return (float)h[1]; }
__device__ __forceinline__ unsigned lds_addr(const void* p) { return (unsigned)(size_t)p; }
__device__ __forceinline__ u32x2 tr_read(unsigned ad) {
  s16x4 v = __builtin_amdgcn_ds_read_tr16_b64_v4i16((LDS_AS s16x4*)(size_t)ad);
  return __builtin_bit_cast(u32x2, v);
}
__device__ __forceinline__ void glds16_sc1(const void* gsrc, unsigned lds_dst) {
  unsigned keep;
  asm volatile("s_mov_b32 %0, m0\n\ts_mov_b32 m0, %2\n\ts_nop 0\n\tglobal_load_lds_dwordx4 %1, off sc1\n\ts_mov_b32 m0, %0"
               : "=&s"(keep) : "v"(gsrc), "s"(lds_dst) : "memory");
}
__device__ __forceinline__ void store8_dev(void* p, u32x2 v) {
  unsigned long long x = ((unsigned long long)v[1] << 32) | v[0];
  __hip_atomic_store((unsigned long long*)p, x, __ATOMIC_RELAXED, __HIP_MEMORY_SCOPE_AGENT);
}
__device__ __forceinline__ void raw_barrier() {
  asm volatile("s_waitcnt lgkmcnt(0)" ::: "memory");
  __builtin_amdgcn_s_barrier();
  asm volatile("" ::: "memory");
}
template <int CTRL>
__device__ __forceinline__ float dpp_add(float v) {
  int r = __builtin_amdgcn_update_dpp(0, __float_as_int(v), CTRL, 0xf, 0xf, false);
  return v + __int_as_float(r);
}
__device__ __forceinline__ float row16_sum(float v) {
  v = dpp_add<0xB1>(v);
  v = dpp_add<0x4E>(v);
  v = dpp_add<0x141>(v);
  v = dpp_add<0x140>(v);
  return v;
}

struct cpx { float r, i; };
__device__ __forceinline__ cpx cmul(cpx a, cpx b) {
  cpx o; o.r = a.r * b.r - a.i * b.i; o.i = a.r * b.i + a.i * b.r; return o;
}
__device__ __forceinline__ cpx cexp_dir(float lr2, float fr, float e) {
  float mag = __builtin_amdgcn_exp2f(lr2 * e);
  float ph = __builtin_amdgcn_fractf(fr * e);
  cpx o; o.r = mag * __builtin_amdgcn_cosf(ph); o.i = mag * __builtin_amdgcn_sinf(ph); return o;
}

#define K2_ROWB 272
#define K2_COPY (18 * K2_ROWB)
#define K2_UCH (4 * K2_COPY)
#define K2_LDS_U 0
#define K2_LDS_K (39 * 1024)
#define K2_LDS_X (K2_LDS_K + 2 * 4608)
#define KG_KCB 1344
#define KG_LDS_KC (K2_LDS_X + 8192)
#define K2_LDS_Y (KG_LDS_KC + 3072)
#define KG_AROW 528
#define KG_LDS_A K2_LDS_Y
#define KG_LDS_B (KG_LDS_A + 2 * 32 * KG_AROW)
#define P1_LDS_W (KG_LDS_B + 2 * 21 * KG_AROW)
#define P1_LDS_RED (P1_LDS_W + 4 * 5120)
#define K3_LDS_H 0
#define K3_LDS_E 65536
#define K3_LDS_ST (K3_LDS_E + 8 * 9216)
#define K3_LDS_SF (K3_LDS_ST + 1024)
#define K4_LDS_R 0
#define KB_LDS_SIZE (K3_LDS_SF + 64)
#define SPIN_MAX (1u << 22)

__device__ __forceinline__ f16x8 ld_g(const char* p) {
  u32x2 lo = *(const u32x2*)p, hi = *(const u32x2*)(p + 8);
  u32x4 t = {lo[0], lo[1], hi[0], hi[1]};
  return __builtin_bit_cast(f16x8, t);
}
__device__ __forceinline__ bf16x8 ld_bfrag(unsigned ad) {
  u32x2 l2 = tr_read(ad), h2 = tr_read(ad + 512);
  u32x4 tt = {l2[0], l2[1], h2[0], h2[1]};
  return __builtin_bit_cast(bf16x8, tt);
}

__device__ __forceinline__ float a_chain(char* Aimg, int ch, const float (&lr2)[2], const float (&fr)[2], unsigned v_cr, unsigned v_ci) {
  float cm = fmaxf(fmaxf(fabsf(bflo(v_cr)), fabsf(bfhi(v_cr))), fmaxf(fabsf(bflo(v_ci)), fabsf(bfhi(v_ci))));
#pragma unroll
  for (int o = 32; o > 0; o >>= 1) cm = fmaxf(cm, __shfl_xor(cm, o));
  const float cinv = cm > 1e-30f ? 1.f / cm : 0.f;
  cpx P[2][2], z16[2];
  const float e0 = 64.f * ch;
#pragma unroll
  for (int m = 0; m < 2; ++m) {
    cpx C; C.r = (m ? bfhi(v_cr) : bflo(v_cr)) * cinv; C.i = (m ? bfhi(v_ci) : bflo(v_ci)) * cinv;
    cpx z8 = cexp_dir(lr2[m], fr[m], 8.f);
    z16[m] = cmul(z8, z8);
    P[m][0] = cmul(C, cexp_dir(lr2[m], fr[m], e0));
    P[m][1] = cmul(P[m][0], z8);
  }
#pragma unroll
  for (int a = 0; a < 4; ++a) {
#pragma unroll
    for (int k = 0; k < 2; ++k) {
      u32x2 o = {pk_f16(P[0][k].r, P[0][k].i), pk_f16(P[1][k].r, P[1][k].i)};
      *(u32x2*)(Aimg + (8 * ch + 2 * a + k) * 528) = o;
      if (a < 3) { P[0][k] = cmul(P[0][k], z16[0]); P[1][k] = cmul(P[1][k], z16[1]); }
    }
  }
  return cm;
}

__device__ __forceinline__ bool flags_ok(u32x4 v, unsigned t0, unsigned t1) {
  return v[0] == t0 && v[1] == t1 && v[2] == t0 && v[3] == t1;
}

extern "C" __global__ __launch_bounds__(512, 2) void fused_main(const u16* __restrict__ x,
                                               const u16* __restrict__ lar, const u16* __restrict__ aim,
                                               const u16* __restrict__ cre, const u16* __restrict__ cim,
                                               const u16* __restrict__ ldt, const u16* __restrict__ dsk,
                                               const u16* __restrict__ w1, const u16* __restrict__ b1,
                                               const u16* __restrict__ w2, const u16* __restrict__ b2,
                                               char* __restrict__ ws, u16* __restrict__ out) {
  __shared__ __attribute__((aligned(16))) char smem[KB_LDS_SIZE];
  const int b = blockIdx.x, tid = threadIdx.x, l = tid & 63;
  const int w = __builtin_amdgcn_readfirstlane(tid >> 6);
  const int g = l >> 4, l15 = l & 15;
  const int cp = (b & 7) * 32 + (b >> 3);
  const int tb = (b & 7) * 4 + ((b >> 3) & 3);
  const int cg = b >> 5;
  const int wl = w & 3, wh = w >> 2;
  const unsigned sb = lds_addr(smem);
  const __amdgpu_buffer_rsrc_t rsy = __builtin_amdgcn_make_buffer_rsrc(ws, 0, 32768, 0x00020000);
  float* red = (float*)(smem + P1_LDS_RED);
  volatile LDS_AS unsigned* lnflag = (volatile LDS_AS unsigned*)(size_t)(sb + P1_LDS_RED + 256);
  const unsigned epoch = __hip_atomic_load((const unsigned*)(ws + WS_EPOCH), __ATOMIC_RELAXED, __HIP_MEMORY_SCOPE_AGENT);
  unsigned xr8[8];
  float piv[8];
  u32x4 wv[2][2];
  const int gw = b * 4 + wl;
  if (wh == 0) {
    const unsigned* xr = (const unsigned*)(x + b * 8 * DM + 128 * wl + 2 * l);
#pragma unroll
    for (int r = 0; r < 8; ++r) xr8[r] = xr[r * 256];
#pragma unroll
    for (int r = 0; r < 8; ++r) piv[r] = bf2f(x[(b * 8 + r) * DM]);
    if (l == 0) lnflag[wl] = 0u;
#pragma unroll
    for (int it = 0; it < 2; ++it) {
      const int bi = gw + it * 1024;
      if (bi < 688) {
        const int ks = bi / 43, fb = bi - ks * 43;
        const u16* src = w1 + (size_t)(32 * ks + (l >> 2)) * FF + 32 * fb + 8 * (l & 3);
        wv[it][0] = *(const u32x4*)src;
        wv[it][1] = *(const u32x4*)(src + 16 * FF);
      } else if (bi < 1376) {
        const int b2 = bi - 688, ks = b2 >> 4, nbk = b2 & 15;
        const u16* src = w2 + (size_t)(32 * ks + (l >> 2)) * DM + 32 * nbk + 8 * (l & 3);
        wv[it][0] = *(const u32x4*)src;
        wv[it][1] = *(const u32x4*)(src + 16 * DM);
      }
    }
  }
  u32x4 xt[2];
  if (wh == 1) {
    const u16* src = x + (size_t)((b >> 3) * 64 + (l >> 1)) * DM + (b & 7) * 64 + wl * 16 + (l & 1) * 8;
    xt[0] = *(const u32x4*)src;
    xt[1] = *(const u32x4*)(src + 32 * DM);
    if (l == 0) lnflag[4 + wl] = 0u;
  }
  const int kc_c = wl >> 1, part = wl & 1;
  const int dch = 2 * cp + kc_c;
  const unsigned v_lar = *(const unsigned*)(lar + dch * NM + 2 * l), v_aim = *(const unsigned*)(aim + dch * NM + 2 * l);
  const unsigned v_cr = *(const unsigned*)(cre + dch * NM + 2 * l), v_ci = *(const unsigned*)(cim + dch * NM + 2 * l);
  const unsigned v_ldt = *(const unsigned*)(ldt + 2 * cp);
  const unsigned dkv = *(const unsigned*)(dsk + 2 * cp);
  if (l < 35) *(u32x4*)(smem + K2_LDS_U + w * K2_COPY + (l < 34 ? l * 16 : 17 * K2_ROWB + 256)) = (u32x4){0u, 0u, 0u, 0u};
  if (wl == 3 && l < 18) *(u32x4*)(smem + K2_LDS_K + wh * 4608 + (l < 3 ? l : 257 + l) * 16) = (u32x4){0u, 0u, 0u, 0u};
  if (wl == 2 && l < 2) *(u32x4*)(smem + KG_LDS_KC + wh * KG_KCB + l * 16) = (u32x4){0u, 0u, 0u, 0u};
  if (w == 1 && l < 2) *(u32x4*)(smem + K3_LDS_SF + l * 16) = (u32x4){0u, 0u, 0u, 0u};
  raw_barrier();
  const unsigned tag0 = epoch + 1u, tag1 = tag0 * 0x9E3779B1u + 0x7F4A7C15u;
  const float dt = rbf(__expf(kc_c ? bfhi(v_ldt) : bflo(v_ldt)));
  float lr2[2], fr[2];
  if (wh == 0) {
    float s[16];
#pragma unroll
    for (int r = 0; r < 8; ++r) {
      float a = bflo(xr8[r]) - piv[r], bb = bfhi(xr8[r]) - piv[r];
      s[r] = a + bb;
      s[8 + r] = a * a + bb * bb;
    }
    float t8[8], t4[4];
#pragma unroll
    for (int i = 0; i < 8; ++i) {
      auto sw = __builtin_amdgcn_permlane32_swap(__float_as_uint(s[i]), __float_as_uint(s[8 + i]), false, false);
      t8[i] = __uint_as_float(sw[0]) + __uint_as_float(sw[1]);
    }
#pragma unroll
    for (int i = 0; i < 4; ++i) {
      auto sw = __builtin_amdgcn_permlane16_swap(__float_as_uint(t8[i]), __float_as_uint(t8[4 + i]), false, false);
      t4[i] = row16_sum(__uint_as_float(sw[0]) + __uint_as_float(sw[1]));
    }
    if (l15 == 0) *(f32x4*)(red + wl * 16 + g * 4) = (f32x4){t4[0], t4[1], t4[2], t4[3]};
    asm volatile("s_waitcnt lgkmcnt(0)" ::: "memory");
    if (l == 0) lnflag[wl] = 1u;
    if (wl == 0) {
#pragma unroll 1
      for (unsigned it = 0; it < (1u << 20); ++it) {
        unsigned f = lnflag[l & 3];
        if (__all(f == 1u)) break;
        __builtin_amdgcn_s_sleep(1);
      }
      asm volatile("" ::: "memory");
      const int r = l & 7;
      float t1 = red[r] + red[16 + r] + red[32 + r] + red[48 + r];
      float t2 = red[8 + r] + red[24 + r] + red[40 + r] + red[56 + r];
      float pv = piv[0];
#pragma unroll
      for (int k = 1; k < 8; ++k) pv = r == k ? piv[k] : pv;
      float dm = t1 * (1.f / 512.f);
      float var = fmaxf(t2 * (1.f / 512.f) - dm * dm, 0.f);
      const unsigned md = pk_bf16(pv + dm, __builtin_amdgcn_sqrtf(rbf(rbf(var) + 1e-5f)));
      if (l < 8) store8_dev(ws + WS_G0 + (b * 8 + l) * 8, (u32x2){md, tag0});
    }
    {
      char* wlp = smem + P1_LDS_W + wl * 5120;
      const __amdgpu_buffer_rsrc_t rwt = __builtin_amdgcn_make_buffer_rsrc(ws + WS_W1T, 0, (NFTP * 16 + 32 * NKSP) * 1024, 0x00020000);
      if (wl == 2 && b < 32) __builtin_amdgcn_raw_buffer_store_b128((u32x4){0u, 0u, 0u, 0u}, rwt, (86 * 16 + b) * 1024 + l * 16, 0, 16);
#pragma unroll
      for (int it = 0; it < 2; ++it) {
        const int bi = gw + it * 1024;
        if (bi < 1376) {
          char* t = wlp + it * 2560;
          *(u32x4*)(t + (l >> 2) * 80 + (l & 3) * 16) = wv[it][0];
          *(u32x4*)(t + (16 + (l >> 2)) * 80 + (l & 3) * 16) = wv[it][1];
          const bool isw1 = bi < 688;
          const int q = l15 >> 2, p = l15 & 3;
          const unsigned a0 = lds_addr(t) + (isw1 ? (8 * g + q) : (4 * g + q)) * 80 + p * 8;
          const unsigned dr = isw1 ? 4 * 80 : 16 * 80;
#pragma unroll
          for (int ct = 0; ct < 2; ++ct) {
            u32x2 lo = tr_read(a0 + ct * 32), hi = tr_read(a0 + ct * 32 + dr);
            u32x4 o = {lo[0], lo[1], hi[0], hi[1]};
            unsigned off;
            if (isw1) { const int ks = bi / 43, fb = bi - ks * 43; off = ((2 * fb + ct) * 16 + ks) * 1024; }
            else { const int b2 = bi - 688, ks = b2 >> 4; off = (NFTP * 16 + (2 * (b2 & 15) + ct) * NKSP + ks) * 1024; }
            __builtin_amdgcn_raw_buffer_store_b128(o, rwt, off + l * 16, 0, 16);
          }
        }
      }
    }
    lr2[0] = (-rbf(__expf(bflo(v_lar))) * dt) * 1.4426950408889634f;
    lr2[1] = (-rbf(__expf(bfhi(v_lar))) * dt) * 1.4426950408889634f;
    fr[0] = (bflo(v_aim) * dt) * 0.15915494309189535f;
    fr[1] = (bfhi(v_aim) * dt) * 0.15915494309189535f;
    const float cm = a_chain(smem + KG_LDS_A + kc_c * 32 * KG_AROW + l * 8, 2 * part, lr2, fr, v_cr, v_ci);
    if (part == 0 && l == 0) red[64 + 8 + kc_c] = 2.f * dt * cm * 128.f;
    asm volatile("s_waitcnt vmcnt(0)" ::: "memory");
    raw_barrier();
    if (tid == 192) store8_dev(ws + WS_FW + b * 8, (u32x2){tag0, tag1});
    {
      const char* Ab = smem + KG_LDS_A + kc_c * 32 * KG_AROW + (16 * part + l15) * KG_AROW + g * 16;
      const char* B0 = smem + KG_LDS_B + kc_c * 21 * KG_AROW + l15 * KG_AROW + g * 16;
      const char* B1 = smem + KG_LDS_B + kc_c * 21 * KG_AROW + (16 + (l15 < 5 ? l15 : 4)) * KG_AROW + g * 16;
      f16x8 af[8], b0[8], b1f[8];
#pragma unroll
      for (int q = 0; q < 8; ++q) { af[q] = *(const f16x8*)(Ab + q * 64); b0[q] = *(const f16x8*)(B0 + q * 64); b1f[q] = *(const f16x8*)(B1 + q * 64); }
      f32x4 acc0 = {0, 0, 0, 0}, acc1 = {0, 0, 0, 0};
#pragma unroll
      for (int q = 0; q < 8; ++q) {
        acc0 = __builtin_amdgcn_mfma_f32_16x16x32_f16(af[q], b0[q], acc0, 0, 0, 0);
        acc1 = __builtin_amdgcn_mfma_f32_16x16x32_f16(af[q], b1f[q], acc1, 0, 0, 0);
      }
      _Float16* kc = (_Float16*)(smem + KG_LDS_KC + kc_c * KG_KCB + 32);
      const int a0 = 16 * part + 4 * g;
      int base, stride;
      if (l15 < 8) { base = l15; stride = 8; }
      else if (l15 < 12) { base = 256 + (l15 - 8); stride = 4; }
      else { base = 384 + 64 * ((l15 - 12) >> 1) + (l15 & 1); stride = 2; }
#pragma unroll
      for (int r = 0; r < 4; ++r) {
        kc[base + stride * (a0 + r)] = (_Float16)(acc0[r] * (1.f / 128.f));
        if (l15 < 4 || (l15 == 4 && a0 + r == 0)) kc[512 + 32 * l15 + a0 + r] = (_Float16)(acc1[r] * (1.f / 128.f));
      }
    }
    raw_barrier();
    u32x4 fwa = {0u, 0u, 0u, 0u}, fwb = {0u, 0u, 0u, 0u};
    if (w == 3) {
      fwa = __builtin_amdgcn_raw_buffer_load_b128(rsy, WS_FW + l * 32, 0, 16);
      fwb = __builtin_amdgcn_raw_buffer_load_b128(rsy, WS_FW + l * 32 + 16, 0, 16);
    }
    __builtin_amdgcn_sched_barrier(0);
    {
      const int c2 = tid + 1;
      int ih; unsigned tbl;
      if (c2 <= 32) { ih = 8 * c2; tbl = 0x76543210u; }
      else if (c2 <= 64) { ih = 256 + 4 * (c2 - 32); tbl = 0x43222100u; }
      else if (c2 <= 128) { ih = 384 + 2 * (c2 - 64); tbl = 0x22111000u; }
      else { ih = 512 + (c2 - 128); tbl = 0x11100000u | ((unsigned)(c2 & 1) << 16); }
#pragma unroll
      for (int c = 0; c < 2; ++c) {
        const u16* kc0 = (const u16*)(smem + KG_LDS_KC + c * KG_KCB + 32);
        const u16* kc = kc0 + ih;
        unsigned v[8];
#pragma unroll
        for (int j = 0; j < 8; ++j) v[j] = kc[-(int)((tbl >> (4 * j)) & 15u)];
        if (c2 == 256) v[0] = 0u;
        u32x4 o = {v[0] | (v[1] << 16), v[2] | (v[3] << 16), v[4] | (v[5] << 16), v[6] | (v[7] << 16)};
        *(u32x4*)(smem + K2_LDS_K + c * 4608 + (259 - c2) * 16) = o;
        if (c2 == 1) *(u32x4*)(smem + K2_LDS_K + c * 4608 + 259 * 16) = (u32x4){(unsigned)kc0[0], 0u, 0u, 0u};
      }
    }
    if (w == 3) {
#pragma unroll 1
      for (unsigned it = 0; it < SPIN_MAX; ++it) {
        if (__all(flags_ok(fwa, tag0, tag1) && flags_ok(fwb, tag0, tag1))) break;
        __builtin_amdgcn_s_sleep(2);
        fwa = __builtin_amdgcn_raw_buffer_load_b128(rsy, WS_FW + l * 32, 0, 16);
        fwb = __builtin_amdgcn_raw_buffer_load_b128(rsy, WS_FW + l * 32 + 16, 0, 16);
      }
    }
  } else {
    const int at = tid - 256;
    const __amdgpu_buffer_rsrc_t rxt = __builtin_amdgcn_make_buffer_rsrc(ws + WS_XT, 0, DM * XT_PITCH, 0x00020000);
    {
      char* tl = smem + K2_LDS_X + wl * 2048;
      *(u32x4*)(tl + l * 16) = xt[0];
      *(u32x4*)(tl + 1024 + l * 16) = xt[1];
      const int q = l15 >> 2, p = l15 & 3;
      const unsigned ta = lds_addr(tl) + (8 * g + q) * 32 + p * 8;
      u32x4 pc[2];
#pragma unroll
      for (int i = 0; i < 2; ++i) {
        u32x2 lo = tr_read(ta + i * 1024), hi = tr_read(ta + i * 1024 + 128);
        pc[i] = (u32x4){lo[0], lo[1], hi[0], hi[1]};
      }
      asm volatile("s_waitcnt lgkmcnt(0)" ::: "memory");
#pragma unroll
      for (int i = 0; i < 2; ++i) *(u32x4*)(tl + l15 * 128 + (g + 4 * i) * 16) = pc[i];
#pragma unroll
      for (int i = 0; i < 2; ++i) {
        const u32x4 o = *(const u32x4*)(tl + i * 1024 + l * 16);
        const int ch = (b & 7) * 64 + wl * 16 + i * 8 + (l >> 3);
        __builtin_amdgcn_raw_buffer_store_b128(o, rxt, ch * XT_PITCH + (b >> 3) * 128 + (l & 7) * 16, 0, 16);
      }
      asm volatile("s_waitcnt vmcnt(0)" ::: "memory");
      if (l == 0) lnflag[4 + wl] = 1u;
      if (wl == 0) {
#pragma unroll 1
        for (unsigned it = 0; it < (1u << 20); ++it) {
          unsigned f = lnflag[4 + (l & 3)];
          if (__all(f == 1u)) break;
          __builtin_amdgcn_s_sleep(1);
        }
        if (l == 0) store8_dev(ws + WS_FX + ((b & 7) * 32 + (b >> 3)) * 8, (u32x2){tag0, tag1});
      }
    }
    lr2[0] = (-rbf(__expf(bflo(v_lar))) * dt) * 1.4426950408889634f;
    lr2[1] = (-rbf(__expf(bfhi(v_lar))) * dt) * 1.4426950408889634f;
    fr[0] = (bflo(v_aim) * dt) * 0.15915494309189535f;
    fr[1] = (bfhi(v_aim) * dt) * 0.15915494309189535f;
    {
      char* Bimg = smem + KG_LDS_B + kc_c * 21 * KG_AROW + l * 8;
#define PUTB(col, v0, v1) { cpx _a = (v0), _b = (v1); u32x2 _o = {pk_f16(_a.r, -_a.i), pk_f16(_b.r, -_b.i)}; *(u32x2*)(Bimg + (col) * KG_AROW) = _o; }
      if (part == 0) {
        cpx z[2][8], e256[2];
#pragma unroll
        for (int m = 0; m < 2; ++m) {
          z[m][0].r = 1.f; z[m][0].i = 0.f;
          z[m][1] = cexp_dir(lr2[m], fr[m], 1.f);
          z[m][2] = cmul(z[m][1], z[m][1]); z[m][3] = cmul(z[m][2], z[m][1]); z[m][4] = cmul(z[m][2], z[m][2]);
          z[m][5] = cmul(z[m][4], z[m][1]); z[m][6] = cmul(z[m][4], z[m][2]); z[m][7] = cmul(z[m][4], z[m][3]);
          e256[m] = cexp_dir(lr2[m], fr[m], 256.f);
        }
#pragma unroll
        for (int k = 0; k < 8; ++k) PUTB(k, z[0][k], z[1][k]);
        PUTB(8, e256[0], e256[1]);
#pragma unroll
        for (int k = 1; k < 4; ++k) PUTB(8 + k, cmul(e256[0], z[0][2 * k]), cmul(e256[1], z[1][2 * k]));
      } else {
        cpx z4[2], e256[2], e512[2], e768[2], e1024[2];
#pragma unroll
        for (int m = 0; m < 2; ++m) {
          z4[m] = cexp_dir(lr2[m], fr[m], 4.f);
          e256[m] = cexp_dir(lr2[m], fr[m], 256.f);
          e1024[m] = cexp_dir(lr2[m], fr[m], 1024.f);
          e512[m] = cmul(e256[m], e256[m]); e768[m] = cmul(e512[m], e256[m]);
        }
        PUTB(12, e512[0], e512[1]); PUTB(13, cmul(e512[0], z4[0]), cmul(e512[1], z4[1]));
        PUTB(14, e768[0], e768[1]); PUTB(15, cmul(e768[0], z4[0]), cmul(e768[1], z4[1]));
        PUTB(16, e1024[0], e1024[1]);
        PUTB(17, cmul(e1024[0], e256[0]), cmul(e1024[1], e256[1]));
        PUTB(18, cmul(e1024[0], e512[0]), cmul(e1024[1], e512[1]));
        PUTB(19, cmul(e1024[0], e768[0]), cmul(e1024[1], e768[1]));
        PUTB(20, cmul(e1024[0], e1024[0]), cmul(e1024[1], e1024[1]));
      }
#undef PUTB
    }
    a_chain(smem + KG_LDS_A + kc_c * 32 * KG_AROW + l * 8, 2 * part + 1, lr2, fr, v_cr, v_ci);
    raw_barrier();
    const int p0 = 8 * at;
    const unsigned upos = ((p0 >> 7) + 2) * K2_ROWB + (p0 & 127) * 2;
    const bool dup = (p0 & 127) == 0;
    u32x4 uo[2];
    {
#pragma unroll 1
      for (unsigned it = 0; it < SPIN_MAX; ++it) {
        u32x4 fa = __builtin_amdgcn_raw_buffer_load_b128(rsy, WS_FX + (cp >> 5) * 256 + (l & 15) * 16, 0, 16);
        if (__all(flags_ok(fa, tag0, tag1))) break;
        __builtin_amdgcn_s_sleep(1);
      }
      asm volatile("" ::: "memory");
      u32x4 xq[2];
#pragma unroll
      for (int c = 0; c < 2; ++c) xq[c] = __builtin_amdgcn_raw_buffer_load_b128(rxt, (2 * cp + c) * XT_PITCH + at * 16, 0, 16);
      u32x4 gq[4];
#pragma unroll 1
      for (unsigned it = 0; it < SPIN_MAX; ++it) {
        bool ok = true;
#pragma unroll
        for (int k = 0; k < 4; ++k) {
          gq[k] = __builtin_amdgcn_raw_buffer_load_b128(rsy, WS_G0 + at * 64 + k * 16, 0, 16);
          ok = ok && gq[k][1] == tag0 && gq[k][3] == tag0;
        }
        if (__all(ok)) break;
        __builtin_amdgcn_s_sleep(1);
      }
#pragma unroll
      for (int k = 0; k < 4; ++k) {
        const float m0 = bflo(gq[k][0]), r0 = __builtin_amdgcn_rcpf(bfhi(gq[k][0]));
        const float m1 = bflo(gq[k][2]), r1 = __builtin_amdgcn_rcpf(bfhi(gq[k][2]));
#pragma unroll
        for (int c = 0; c < 2; ++c) {
          unsigned dd = pk_bf16(bflo(xq[c][k]) - m0, bfhi(xq[c][k]) - m1);
          unsigned uu = pk_bf16(bflo(dd) * r0, bfhi(dd) * r1);
          uo[c][k] = pk_f16(bflo(uu), bfhi(uu));
        }
      }
#pragma unroll
      for (int c = 0; c < 2; ++c) {
        *(u32x4*)(smem + K2_LDS_X + c * 4096 + p0 * 2) = xq[c];
        *(u32x4*)(smem + K2_LDS_U + c * K2_UCH + upos) = uo[c];
        if (dup) *(u32x4*)(smem + K2_LDS_U + c * K2_UCH + upos - K2_ROWB + 256) = uo[c];
      }
    }
    raw_barrier();
#pragma unroll
    for (int c = 0; c < 2; ++c) {
      const u32x2 nx = *(const u32x2*)(smem + K2_LDS_U + c * K2_UCH + upos + 16);
      const unsigned d[6] = {uo[c][0], uo[c][1], uo[c][2], uo[c][3], nx[0], nx[1]};
      unsigned e[5];
#pragma unroll
      for (int i = 0; i < 5; ++i) e[i] = __builtin_amdgcn_alignbit(d[i + 1], d[i], 16);
      const u32x4 c1 = {e[0], e[1], e[2], e[3]}, c2 = {d[1], d[2], d[3], d[4]}, c3 = {e[1], e[2], e[3], e[4]};
      char* dst = smem + K2_LDS_U + c * K2_UCH + upos;
      *(u32x4*)(dst + 1 * K2_COPY) = c1;
      *(u32x4*)(dst + 2 * K2_COPY) = c2;
      *(u32x4*)(dst + 3 * K2_COPY) = c3;
      if (dup) {
        *(u32x4*)(dst + 1 * K2_COPY - K2_ROWB + 256) = c1;
        *(u32x4*)(dst + 2 * K2_COPY - K2_ROWB + 256) = c2;
        *(u32x4*)(dst + 3 * K2_COPY - K2_ROWB + 256) = c3;
      }
      if (at == 0) {
        char* dn = smem + K2_LDS_U + c * K2_UCH + K2_ROWB + 124 * 2;
        *(u32x2*)(dn + 1 * K2_COPY) = (u32x2){0u, d[0] << 16};
        *(u32x2*)(dn + 2 * K2_COPY) = (u32x2){0u, d[0]};
        *(u32x2*)(dn + 3 * K2_COPY) = (u32x2){d[0] << 16, e[0]};
      }
    }
  }
  raw_barrier();
  const float ksc = red[64 + 8 + wh];
  const int myft = cg * 11 + 3 * wl;
  bf16x8 wf[3][8];
  const __amdgpu_buffer_rsrc_t rw1 = __builtin_amdgcn_make_buffer_rsrc(ws + WS_W1T, 0, NFTP * 16 * 1024, 0x00020000);
  const unsigned w1o = (myft * 16 + wh * 8) * 1024;
#define LDW1(a, k) __builtin_bit_cast(bf16x8, __builtin_amdgcn_raw_buffer_load_b128(rw1, l * 16, w1o + (a) * 16 * 1024 + (k) * 1024, 16))
  const u32x2 b2v = *(const u32x2*)(b2 + cg * 64 + (w >> 1) * 16 + 4 * g);
  u32x2 b1v[3];
#pragma unroll
  for (int a = 0; a < 3; ++a) {
    const int f = 16 * (myft + a) + 4 * g;
    b1v[a] = *(const u32x2*)(b1 + (f < FF ? f : 0));
  }
  {
    f32x4 acc[8];
#pragma unroll
    for (int i = 0; i < 8; ++i) acc[i] = (f32x4){0, 0, 0, 0};
    const int phi = l15 & 7, nu = l15 >> 3;
    const char* Fb = smem + K2_LDS_K + wh * 4608 + (259 - l15 + g) * 16 + 192 - 64 * wl;
    const char* Gb = smem + K2_LDS_U + wh * K2_UCH + (phi & 3) * K2_COPY + (nu + 2) * K2_ROWB + ((phi & 4) + 8 * g) * 2 + 192 - 64 * wl;
    f16x8 F[2][8], G[2][8];
#pragma unroll
    for (int q = 0; q < 8; ++q) { G[0][q] = ld_g(Gb + q * 2 * K2_ROWB); F[0][q] = *(const f16x8*)(Fb - q * 512); }
    __builtin_amdgcn_sched_barrier(0);
#pragma unroll
    for (int rr = 0; rr < 2; ++rr) {
      if (rr == 0) {
#pragma unroll
        for (int q = 0; q < 8; ++q) { G[1][q] = ld_g(Gb - K2_ROWB + q * 2 * K2_ROWB); F[1][q] = *(const f16x8*)(Fb - 256 - q * 512); }
      }
#pragma unroll
      for (int k = 0; k < 8; ++k) wf[rr][k] = LDW1(rr, k);
#pragma unroll
      for (int e = 0; e < 8; ++e)
#pragma unroll
        for (int q = 0; q + e < 8; ++q)
          acc[q + e] = __builtin_amdgcn_mfma_f32_16x16x32_f16(G[rr][e], F[rr][q], acc[q + e], 0, 0, 0);
#pragma unroll
      for (int k = 0; k < 8; ++k) {
        __builtin_amdgcn_sched_group_barrier(0x008, 2, 0);
        if (rr == 0) __builtin_amdgcn_sched_group_barrier(0x100, 1, 0);
        __builtin_amdgcn_sched_group_barrier(0x008, 2, 0);
        if (rr == 0) __builtin_amdgcn_sched_group_barrier(0x100, 1, 0);
        __builtin_amdgcn_sched_group_barrier(0x020, 1, 0);
      }
      __builtin_amdgcn_sched_barrier(0);
      if (rr == 0 && wl == 0) {
        f16x8 Gm = ld_g(Gb - 2 * K2_ROWB), F8 = *(const f16x8*)(Fb - 8 * 512);
#pragma unroll
        for (int i = 0; i < 7; ++i) acc[i] = __builtin_amdgcn_mfma_f32_16x16x32_f16(Gm, F[0][i + 1], acc[i], 0, 0, 0);
        acc[7] = __builtin_amdgcn_mfma_f32_16x16x32_f16(Gm, F8, acc[7], 0, 0, 0);
      }
    }
#pragma unroll
    for (int i = 0; i < 8; ++i) *(f32x4*)(smem + K2_LDS_Y + (w * 8 + i) * 1024 + l * 16) = acc[i];
    raw_barrier();
    const float dk = wh ? bfhi(dkv) : bflo(dkv);
    const int tl = 128 * (g >> 1) + 8 * l15 + 4 * (g & 1);
#pragma unroll
    for (int i = 0; i < 2; ++i) {
      const int ti = 2 * wl + i;
      f32x4 o = *(const f32x4*)(smem + K2_LDS_Y + ((wh * 4 + 0) * 8 + ti) * 1024 + l * 16);
#pragma unroll
      for (int k = 1; k < 4; ++k) o += *(const f32x4*)(smem + K2_LDS_Y + ((wh * 4 + k) * 8 + ti) * 1024 + l * 16);
      const int t0 = 256 * ti + tl;
      u32x2 uu = *(const u32x2*)(smem + K2_LDS_U + wh * K2_UCH + ((t0 >> 7) + 2) * K2_ROWB + (t0 & 127) * 2);
      u32x2 xx = *(const u32x2*)(smem + K2_LDS_X + wh * 4096 + t0 * 2);
      u32x2 res;
#pragma unroll
      for (int j = 0; j < 2; ++j) {
        unsigned yy = pk_bf16(o[2 * j] * ksc, o[2 * j + 1] * ksc);
        unsigned ud = pk_bf16(h2f_lo(uu[j]) * dk, h2f_hi(uu[j]) * dk);
        unsigned z = pk_bf16(bflo(yy) + bflo(ud), bfhi(yy) + bfhi(ud));
        res[j] = pk_bf16(bflo(xx[j]) + bflo(z), bfhi(xx[j]) + bfhi(z));
      }
      store8_dev(ws + WS_HT + (size_t)(2 * cp + wh) * XT_PITCH + t0 * 2, res);
    }
  }
  asm volatile("s_waitcnt vmcnt(0)" ::: "memory");
  raw_barrier();
  if (tid == 192) store8_dev(ws + WS_F1 + b * 8, (u32x2){tag0, tag1});
  const int ks0 = w * 5 + (w < 3 ? w : 3);
  bf16x8 w2f[6][4];
  const __amdgpu_buffer_rsrc_t rw2 = __builtin_amdgcn_make_buffer_rsrc(ws + WS_W2T, 0, 32 * NKSP * 1024, 0x00020000);
  const unsigned w2o = ((cg * 4) * NKSP + ks0) * 1024;
#define LDW2(k, a) __builtin_bit_cast(bf16x8, __builtin_amdgcn_raw_buffer_load_b128(rw2, l * 16, w2o + ((a) * NKSP + (k)) * 1024, 16))
  if (wl < 3) {
#pragma unroll
    for (int k = 0; k < 8; ++k) wf[2][k] = LDW1(2, k);
  } else {
#pragma unroll
    for (int k = 0; k < 8; ++k) wf[2][k] = wf[1][k];
  }
  __builtin_amdgcn_sched_barrier(0);
  const u32x4 onesu = {0x3f803f80u, 0x3f803f80u, 0x3f803f80u, 0x3f803f80u};
  const bf16x8 onesf = __builtin_bit_cast(bf16x8, onesu);
  {
    f32x4 cq[2];
#pragma unroll
    for (int a = 0; a < 2; ++a) cq[a] = (f32x4){0, 0, 0, 0};
#pragma unroll
    for (int k = 0; k < 8; ++k)
#pragma unroll
      for (int a = 0; a < 2; ++a) cq[a] = __builtin_amdgcn_mfma_f32_16x16x32_bf16(wf[a][k], onesf, cq[a], 0, 0, 0);
#pragma unroll
    for (int a = 0; a < 2; ++a) *(f32x4*)(smem + K3_LDS_E + (w * 9 + 6 + a) * 1024 + l * 16) = cq[a];
  }
  if (w == 3) {
#pragma unroll 1
    for (unsigned it = 0; it < SPIN_MAX; ++it) {
      u32x4 fa = __builtin_amdgcn_raw_buffer_load_b128(rsy, WS_F1 + l * 32, 0, 16);
      u32x4 fb = __builtin_amdgcn_raw_buffer_load_b128(rsy, WS_F1 + l * 32 + 16, 0, 16);
      if (__all(flags_ok(fa, tag0, tag1) && flags_ok(fb, tag0, tag1))) break;
      __builtin_amdgcn_s_sleep(1);
    }
    if (b == 0 && l == 0) __hip_atomic_store((unsigned*)(ws + WS_EPOCH), tag0, __ATOMIC_RELAXED, __HIP_MEMORY_SCOPE_AGENT);
  }
  raw_barrier();
  asm volatile("" : "+v"(wf[2][0]), "+v"(wf[2][1]), "+v"(wf[2][2]), "+v"(wf[2][3]), "+v"(wf[2][4]), "+v"(wf[2][5]), "+v"(wf[2][6]), "+v"(wf[2][7]) : "v"(b2v), "v"(b1v[0]), "v"(b1v[1]), "v"(b1v[2]) : "memory");
#pragma unroll
  for (int it = 0; it < 8; ++it) {
    const int j = it * 8 + w;
    const int row = j * 8 + (l >> 3), pc = l & 7;
    const int swz = ((row >> 1) & 1) | (((row >> 3) & 1) << 1);
    const int lc = (((pc >> 1) ^ swz) << 1) | (pc & 1);
    glds16_sc1(ws + WS_HT + (size_t)row * XT_PITCH + tb * 128 + lc * 16, sb + K3_LDS_H + j * 1024);
  }
  {
    f32x4 cq = {0, 0, 0, 0};
#pragma unroll
    for (int k = 0; k < 8; ++k) cq = __builtin_amdgcn_mfma_f32_16x16x32_bf16(wf[2][k], onesf, cq, 0, 0, 0);
    *(f32x4*)(smem + K3_LDS_E + (w * 9 + 8) * 1024 + l * 16) = cq;
  }
  asm volatile("s_waitcnt vmcnt(0)" ::: "memory");
  raw_barrier();
  const int q4 = l15 >> 2, p4 = l15 & 3;
  const __amdgpu_buffer_rsrc_t rs = __builtin_amdgcn_make_buffer_rsrc(ws + WS_HACT, 0, 64 * NFTP * 1024, 0x00020000);
  u32x2 hres[2];
  {
    const int swz = ((q4 >> 1) & 1) | ((g & 1) << 1);
    const unsigned lbase = sb + K3_LDS_H + wh * 8 * 4096 + (8 * g + q4) * 128 + p4 * 8;
    unsigned ad[4];
#pragma unroll
    for (int t = 0; t < 4; ++t) ad[t] = lbase + (((t ^ (2 * wh)) ^ swz) << 5);
    const unsigned sa = lbase + ((wl ^ swz) << 5);
    const unsigned vo2 = wl < 3 ? (unsigned)(l * 16) : 0x7f000000u;
    f32x4 acc[3][2];
    bf16x8 bf[2][2];
    auto finish = [&](int p) {
      f32x4 cs[3], bb[3], co[3], pa[3][2];
      f32x2 st[2];
#pragma unroll
      for (int t = 0; t < 2; ++t) st[t] = *(const f32x2*)(smem + K3_LDS_ST + ((2 * p + t) * 16 + l15) * 8);
#pragma unroll
      for (int a = 0; a < 3; ++a) {
        cs[a] = *(const f32x4*)(smem + K3_LDS_E + (w * 9 + 6 + a) * 1024 + l * 16);
        co[a] = *(const f32x4*)(smem + K3_LDS_E + ((w ^ 4) * 9 + 6 + a) * 1024 + l * 16);
#pragma unroll
        for (int t = 0; t < 2; ++t) pa[a][t] = *(const f32x4*)(smem + K3_LDS_E + ((w ^ 4) * 9 + a * 2 + t) * 1024 + l * 16);
      }
      __builtin_amdgcn_sched_barrier(0);
#pragma unroll
      for (int a = 0; a < 3; ++a) {
        bb[a] = (f32x4){bflo(b1v[a][0]), bfhi(b1v[a][0]), bflo(b1v[a][1]), bfhi(b1v[a][1])};
        cs[a] += co[a];
#pragma unroll
        for (int t = 0; t < 2; ++t) acc[a][t] += pa[a][t];
      }
      const unsigned so = ((tb * 2 + p) * NFTP + myft) * 1024;
      const f32x2 c1 = {-0.10294324f, -0.10294324f}, c0 = {-2.30220819f, -2.30220819f}, one = {1.f, 1.f};
      f32x2 v[12], q[12];
#pragma unroll
      for (int i = 0; i < 12; ++i) {
        const int a = i >> 2, t = (i >> 1) & 1, r = 2 * (i & 1);
        const f32x2 a2 = {st[t][0], st[t][0]}, b2 = {st[t][1], st[t][1]};
        v[i] = __builtin_elementwise_fma(a2, (f32x2){acc[a][t][r], acc[a][t][r + 1]},
                                         __builtin_elementwise_fma(b2, (f32x2){cs[a][r], cs[a][r + 1]}, (f32x2){bb[a][r], bb[a][r + 1]}));
      }
      __builtin_amdgcn_sched_barrier(0);
#pragma unroll
      for (int i = 0; i < 12; ++i) q[i] = v[i] * v[i];
      __builtin_amdgcn_sched_barrier(0);
#pragma unroll
      for (int i = 0; i < 12; ++i) q[i] = __builtin_elementwise_fma(q[i], c1, c0);
      __builtin_amdgcn_sched_barrier(0);
#pragma unroll
      for (int i = 0; i < 12; ++i) q[i] = q[i] * v[i];
      __builtin_amdgcn_sched_barrier(0);
#pragma unroll
      for (int i = 0; i < 12; ++i) q[i] = (f32x2){__builtin_amdgcn_exp2f(q[i][0]), __builtin_amdgcn_exp2f(q[i][1])};
      __builtin_amdgcn_sched_barrier(0);
#pragma unroll
      for (int i = 0; i < 12; ++i) q[i] = q[i] + one;
      __builtin_amdgcn_sched_barrier(0);
#pragma unroll
      for (int i = 0; i < 12; ++i) q[i] = (f32x2){__builtin_amdgcn_rcpf(q[i][0]), __builtin_amdgcn_rcpf(q[i][1])};
      __builtin_amdgcn_sched_barrier(0);
#pragma unroll
      for (int i = 0; i < 12; ++i) v[i] = v[i] * q[i];
      __builtin_amdgcn_sched_barrier(0);
#pragma unroll
      for (int a = 0; a < 3; ++a) {
        u32x4 o = {pk_bf16(v[4 * a][0], v[4 * a][1]), pk_bf16(v[4 * a + 1][0], v[4 * a + 1][1]),
                   pk_bf16(v[4 * a + 2][0], v[4 * a + 2][1]), pk_bf16(v[4 * a + 3][0], v[4 * a + 3][1])};
        __builtin_amdgcn_raw_buffer_store_b128(o, rs, (a < 2 ? (unsigned)(l * 16) : vo2) + so + a * 1024, 0, 16);
      }
      __builtin_amdgcn_sched_barrier(0);
    };
    volatile LDS_AS unsigned* sflag = (volatile LDS_AS unsigned*)(size_t)(sb + K3_LDS_SF);
    {
      if (wh == 0) {
        f32x4 sS = {0, 0, 0, 0}, sQ = {0, 0, 0, 0};
        bf16x8 sf[16];
#pragma unroll
        for (int ks = 0; ks < 16; ++ks) sf[ks] = ld_bfrag(sa + ks * 4096);
#pragma unroll
        for (int ks = 0; ks < 16; ++ks) {
          sS = __builtin_amdgcn_mfma_f32_16x16x32_bf16(onesf, sf[ks], sS, 0, 0, 0);
          sQ = __builtin_amdgcn_mfma_f32_16x16x32_bf16(sf[ks], sf[ks], sQ, 0, 0, 0);
        }
        float dq = (l15 & 2) ? ((l15 & 1) ? sQ[3] : sQ[2]) : ((l15 & 1) ? sQ[1] : sQ[0]);
        float s2 = __shfl(dq, l15 + 16 * (l15 >> 2));
        float mean = sS[0] * (1.f / 512.f);
        float var = fmaxf(s2 * (1.f / 512.f) - mean * mean, 0.f);
        float m1 = rbf(mean);
        float r1 = __builtin_amdgcn_rcpf(rbf(__builtin_amdgcn_sqrtf(rbf(rbf(var) + 1e-5f))));
        float mean2 = (mean - m1) * r1, var2 = var * r1 * r1;
        float m2 = rbf(mean2);
        float r2 = __builtin_amdgcn_rcpf(rbf(__builtin_amdgcn_sqrtf(rbf(rbf(var2) + 1e-5f))));
        if (l < 16) *(f32x2*)(smem + K3_LDS_ST + (wl * 16 + l) * 8) = (f32x2){r1 * r2, -(m1 * r1 + m2) * r2};
      }
#pragma unroll
      for (int a = 0; a < 3; ++a) { acc[a][0] = (f32x4){0, 0, 0, 0}; acc[a][1] = (f32x4){0, 0, 0, 0}; }
      bf[0][0] = ld_bfrag(ad[2]); bf[0][1] = ld_bfrag(ad[3]);
      __builtin_amdgcn_sched_barrier(0);
#pragma unroll
      for (int ks = 0; ks < 8; ++ks) {
        const int cur = ks & 1, nx = cur ^ 1;
        if (ks < 7) {
          bf[nx][0] = ld_bfrag(ad[2] + (ks + 1) * 4096); bf[nx][1] = ld_bfrag(ad[3] + (ks + 1) * 4096);
        } else {
          bf[nx][0] = ld_bfrag(ad[0]); bf[nx][1] = ld_bfrag(ad[1]);
        }
        w2f[ks >> 2][ks & 3] = LDW2(ks >> 2, ks & 3);
#pragma unroll
        for (int t = 0; t < 2; ++t)
#pragma unroll
          for (int a = 0; a < 3; ++a) acc[a][t] = __builtin_amdgcn_mfma_f32_16x16x32_bf16(wf[a][ks], bf[cur][t], acc[a][t], 0, 0, 0);
#pragma unroll
        for (int k = 0; k < 4; ++k) {
          __builtin_amdgcn_sched_group_barrier(0x008, 1, 0);
          __builtin_amdgcn_sched_group_barrier(0x100, 1, 0);
          if (k == 0) __builtin_amdgcn_sched_group_barrier(0x020, 1, 0);
        }
        __builtin_amdgcn_sched_barrier(0);
      }
#pragma unroll
      for (int a = 0; a < 3; ++a)
#pragma unroll
        for (int t = 0; t < 2; ++t) *(f32x4*)(smem + K3_LDS_E + (w * 9 + a * 2 + t) * 1024 + l * 16) = acc[a][t];
      asm volatile("s_waitcnt lgkmcnt(0)" ::: "memory");
      if (l == 0) sflag[w] = 1u;
    }
    {
#pragma unroll
      for (int a = 0; a < 3; ++a) { acc[a][0] = (f32x4){0, 0, 0, 0}; acc[a][1] = (f32x4){0, 0, 0, 0}; }
      __builtin_amdgcn_sched_barrier(0);
#pragma unroll
      for (int ks = 0; ks < 8; ++ks) {
        const int cur = ks & 1, nx = cur ^ 1;
        if (ks < 7) { bf[nx][0] = ld_bfrag(ad[0] + (ks + 1) * 4096); bf[nx][1] = ld_bfrag(ad[1] + (ks + 1) * 4096); }
        w2f[2 + (ks >> 2)][ks & 3] = LDW2(2 + (ks >> 2), ks & 3);
#pragma unroll
        for (int t = 0; t < 2; ++t)
#pragma unroll
          for (int a = 0; a < 3; ++a) acc[a][t] = __builtin_amdgcn_mfma_f32_16x16x32_bf16(wf[a][ks], bf[cur][t], acc[a][t], 0, 0, 0);
#pragma unroll
        for (int k = 0; k < 4; ++k) {
          __builtin_amdgcn_sched_group_barrier(0x008, 1, 0);
          if (ks < 7) __builtin_amdgcn_sched_group_barrier(0x100, 1, 0);
          if (k == 0) __builtin_amdgcn_sched_group_barrier(0x020, 1, 0);
        }
        __builtin_amdgcn_sched_barrier(0);
      }
    }
#pragma unroll
    for (int a = 0; a < 4; ++a) w2f[4][a] = LDW2(4, a);
    if (w < 3) {
#pragma unroll
      for (int a = 0; a < 4; ++a) w2f[5][a] = LDW2(5, a);
    }
    {
      const int row = cg * 64 + (w >> 1) * 16 + 4 * g + q4;
      const int swz2 = ((row >> 1) & 1) | (((row >> 3) & 1) << 1);
      const unsigned hb = sb + K3_LDS_H + row * 128 + p4 * 8;
#pragma unroll
      for (int i = 0; i < 2; ++i) hres[i] = tr_read(hb + (((2 * i + (w & 1)) ^ swz2) << 5));
    }
#pragma unroll 1
    for (unsigned it = 0; it < (1u << 20); ++it) {
      unsigned f = sflag[l & 7];
      if (__all(f == 1u)) break;
      __builtin_amdgcn_s_sleep(2);
    }
    asm volatile("" ::: "memory");
    finish(wh);
  }
  asm volatile("s_waitcnt vmcnt(0)" ::: "memory");
  raw_barrier();
  if (w == 0) {
    if (l == 0) store8_dev(ws + WS_F2 + tb * 128 + cg * 8, (u32x2){tag0, tag1});
#pragma unroll 1
    for (unsigned it = 0; it < SPIN_MAX; ++it) {
      u32x4 fa = __builtin_amdgcn_raw_buffer_load_b128(rsy, WS_F2 + tb * 128 + (l & 3) * 16, 0, 16);
      if (__all(flags_ok(fa, tag0, tag1))) break;
      __builtin_amdgcn_s_sleep(1);
    }
  }
  raw_barrier();
  {
    const unsigned hvo = ((tb * 2) * NFTP + ks0 * 2) * 1024;
    u32x4 hv[2][6][2];
#pragma unroll
    for (int p = 0; p < 2; ++p) {
#pragma unroll
      for (int k = 0; k < 5; ++k)
#pragma unroll
        for (int f = 0; f < 2; ++f)
          hv[p][k][f] = __builtin_amdgcn_raw_buffer_load_b128(rs, l * 16, hvo + (p * NFTP + 2 * k + f) * 1024, 16);
    }
    if (w < 3) {
#pragma unroll
      for (int p = 0; p < 2; ++p)
#pragma unroll
        for (int f = 0; f < 2; ++f)
          hv[p][5][f] = __builtin_amdgcn_raw_buffer_load_b128(rs, l * 16, hvo + (p * NFTP + 10 + f) * 1024, 16);
    }
    __builtin_amdgcn_sched_barrier(0);
#pragma unroll
    for (int p = 0; p < 2; ++p) {
      f32x4 acc[4][2];
#pragma unroll
      for (int a = 0; a < 4; ++a) { acc[a][0] = (f32x4){0, 0, 0, 0}; acc[a][1] = (f32x4){0, 0, 0, 0}; }
#pragma unroll
      for (int k = 0; k < 6; ++k) {
        if (k < 5 || w < 3) {
          u32x4 x0 = {hv[p][k][0][0], hv[p][k][0][1], hv[p][k][1][0], hv[p][k][1][1]};
          u32x4 x1 = {hv[p][k][0][2], hv[p][k][0][3], hv[p][k][1][2], hv[p][k][1][3]};
          bf16x8 h0 = __builtin_bit_cast(bf16x8, x0), h1 = __builtin_bit_cast(bf16x8, x1);
#pragma unroll
          for (int a = 0; a < 4; ++a) {
            acc[a][0] = __builtin_amdgcn_mfma_f32_16x16x32_bf16(w2f[k][a], h0, acc[a][0], 0, 0, 0);
            acc[a][1] = __builtin_amdgcn_mfma_f32_16x16x32_bf16(w2f[k][a], h1, acc[a][1], 0, 0, 0);
          }
        }
      }
      __builtin_amdgcn_sched_barrier(0);
#pragma unroll
      for (int a = 0; a < 4; ++a)
#pragma unroll
        for (int t = 0; t < 2; ++t) *(f32x4*)(smem + K4_LDS_R + ((w * 4 + a) * 4 + 2 * p + t) * 1024 + l * 16) = acc[a][t];
      __builtin_amdgcn_sched_barrier(0);
    }
    raw_barrier();
    const float bq[4] = {bflo(b2v[0]), bfhi(b2v[0]), bflo(b2v[1]), bfhi(b2v[1])};
#pragma unroll
    for (int i = 0; i < 2; ++i) {
      const int t = 2 * i + (w & 1);
      f32x4 sacc = *(const f32x4*)(smem + K4_LDS_R + ((0 * 4 + (w >> 1)) * 4 + t) * 1024 + l * 16);
#pragma unroll
      for (int ww = 1; ww < 8; ++ww) sacc += *(const f32x4*)(smem + K4_LDS_R + ((ww * 4 + (w >> 1)) * 4 + t) * 1024 + l * 16);
      float o[4];
      o[0] = bflo(hres[i][0]) + rbf(rbf(sacc[0]) + bq[0]);
      o[1] = bfhi(hres[i][0]) + rbf(rbf(sacc[1]) + bq[1]);
      o[2] = bflo(hres[i][1]) + rbf(rbf(sacc[2]) + bq[2]);
      o[3] = bfhi(hres[i][1]) + rbf(rbf(sacc[3]) + bq[3]);
      u32x2 pk = {pk_bf16(o[0], o[1]), pk_bf16(o[2], o[3])};
      *(u32x2*)(out + (size_t)(tb * 64 + t * 16 + l15) * DM + cg * 64 + (w >> 1) * 16 + 4 * g) = pk;
    }
  }
}

extern "C" void kernel_launch(void* const* d_in, const int* in_sizes, int n_in,
                              void* d_out, int out_size, void* d_ws, size_t ws_size,
                              hipStream_t stream) {
  fused_main<<<256, 512, 0, stream>>>((const u16*)d_in[0], (const u16*)d_in[1], (const u16*)d_in[2], (const u16*)d_in[3],
                                      (const u16*)d_in[4], (const u16*)d_in[5], (const u16*)d_in[6], (const u16*)d_in[8],
                                      (const u16*)d_in[9], (const u16*)d_in[10], (const u16*)d_in[11], (char*)d_ws, (u16*)d_out);
}
```
